```python
import torch

def _softmax(x, axis=-1):
    return torch.softmax(x, dim=axis)
def _gelu(x):
    return torch.nn.functional.gelu(x, approximate="tanh")
def _silu(x):
    return torch.nn.functional.silu(x)
def _sigmoid(x):
    return torch.sigmoid(x)
def _layernorm(x, axis=-1, eps=1e-5):
    m = x.mean(dim=axis, keepdim=True)
    v = x.var(dim=axis, keepdim=True, unbiased=False)
    return (x - m) / torch.sqrt(v + eps)
def _scatter_add(tgt, idx, src, axis=0):
    return tgt.clone().index_add_(axis, idx, src)
def _segment_sum(src, seg, n):
    out = torch.zeros((n,) + src.shape[1:], dtype=src.dtype)
    return out.index_add_(0, seg, src)
def _pad(x, pw):
    flat = tuple(v for pair in reversed(pw) for v in pair)
    return torch.nn.functional.pad(x, flat)

kernel_name = 'standalone__paged_block_table_kv_radixattention__sampling'

def setup_inputs():

    _w = (lambda a: (lambda t: t.to(torch.bfloat16) if t.is_floating_point() else t)(torch.as_tensor(a)))
    B, H, Hkv, Dh = 8, 32, 8, 128
    Lk = 2048
    bs = 16
    nb = max(2, Lk // bs)
    P = max(1, nb // 2)
    n_phys = P + B * (nb - P)
    shared = torch.arange(P, dtype=torch.int64)
    priv = P + torch.arange(B * (nb - P), dtype=torch.int64).reshape(B, nb - P)
    bt = torch.cat([torch.tile(shared[None, :], (B, 1)), priv], dim=1)
    ctx = nb * bs - (torch.arange(1, B + 1, dtype=torch.int64) * 2654435761) % max(1, (nb * bs - P * bs) // 2)
    return {
        "q": _w((lambda *s: torch.randn(*s, dtype=torch.bfloat16) * 0.8)(B, H, Dh)),
        "k_cache": _w((lambda *s: torch.randn(*s, dtype=torch.bfloat16) * 0.8)(n_phys, bs, Hkv, Dh)),
        "v_cache": _w((lambda *s: torch.randn(*s, dtype=torch.bfloat16) * 0.05)(n_phys, bs, Hkv, Dh)),
        "block_table": _w(bt.to(torch.int64)),
        "ctx_lens": _w(torch.as_tensor(ctx).to(torch.int64)),
    }

def reference(q, k_cache, v_cache, block_table, ctx_lens):
    B, H, Dh = q.shape
    n_phys, bs, Hkv, _ = k_cache.shape
    nb = block_table.shape[1]
    Lk = nb * bs
    rep = H // Hkv
    k = k_cache[block_table].reshape(B, Lk, Hkv, Dh)
    v = v_cache[block_table].reshape(B, Lk, Hkv, Dh)
    k = (lambda x, n, axis: torch.repeat_interleave(x, n, dim=axis))(k, rep, axis=2)
    v = (lambda x, n, axis: torch.repeat_interleave(x, n, dim=axis))(v, rep, axis=2)
    scores = torch.einsum("bhd,bkhd->bhk", q, k) * (Dh ** -0.5)
    pos = torch.arange(Lk)
    valid = pos[None, :] < ctx_lens[:, None]
    scores = torch.where(valid[:, None, :], scores, -1e9)
    p = _softmax(scores, axis=-1)
    return torch.einsum("bhk,bkhd->bhd", p, v)
```

```cpp
#include <hip/hip_runtime.h>
#include <hip/hip_bf16.h>
#include <stdint.h>

namespace {

constexpr int B_ = 8, H_ = 32, HKV = 8, D_ = 128, BS = 16, NB = 128, LK = NB * BS, G_ = H_ / HKV;
constexpr int NSPLIT = 4, NWAVE = 16, UNIT = 32;
static_assert(NSPLIT * NWAVE * UNIT == LK, "units cover the sequence");

using bf16x8 = __attribute__((ext_vector_type(8))) short;
using f32x4 = __attribute__((ext_vector_type(4))) float;
using u32x4 = __attribute__((ext_vector_type(4))) unsigned;

__device__ __forceinline__ unsigned cvtpk(float lo, float hi) {
  unsigned r;
  asm("v_cvt_pk_bf16_f32 %0, %1, %2" : "=v"(r) : "v"(lo), "v"(hi));
  return r;
}
__device__ __forceinline__ float xg_max(float x) {
  auto s = __builtin_amdgcn_permlane16_swap(__float_as_uint(x), __float_as_uint(x), false, false);
  x = fmaxf(__uint_as_float(s[0]), __uint_as_float(s[1]));
  auto t = __builtin_amdgcn_permlane32_swap(__float_as_uint(x), __float_as_uint(x), false, false);
  return fmaxf(__uint_as_float(t[0]), __uint_as_float(t[1]));
}
__device__ __forceinline__ float xg_sum(float x) {
  auto s = __builtin_amdgcn_permlane16_swap(__float_as_uint(x), __float_as_uint(x), false, false);
  x = __uint_as_float(s[0]) + __uint_as_float(s[1]);
  auto t = __builtin_amdgcn_permlane32_swap(__float_as_uint(x), __float_as_uint(x), false, false);
  return __uint_as_float(t[0]) + __uint_as_float(t[1]);
}

__global__ void __launch_bounds__(1024)
attn_main(const uint16_t* __restrict__ q, const uint16_t* __restrict__ kc, const uint16_t* __restrict__ vc,
          const int* __restrict__ bt, const int* __restrict__ ctxl, float* __restrict__ ws_o,
          float* __restrict__ ws_ml) {
  __shared__ __attribute__((aligned(16))) float lds_o[NWAVE][G_][D_];
  __shared__ __attribute__((aligned(16))) float lds_m[NWAVE][G_];
  __shared__ __attribute__((aligned(16))) float lds_l[NWAVE][G_];

  const int bid = blockIdx.x;
  const int kvh = bid & 7;
  const int b = (bid >> 3) & 7;
  const int split = bid >> 6;
  const int tid = threadIdx.x;
  const int lane = tid & 63;
  const int wave = __builtin_amdgcn_readfirstlane(tid >> 6);
  const int g = lane >> 4, c = lane & 15;

  const int unit = wave * NSPLIT + split;
  const int tok0 = unit * UNIT;
  const int ctx = ctxl[b];
  const int p0 = bt[b * NB + unit * 2];
  const int p1 = bt[b * NB + unit * 2 + 1];
  const bool zeroq = ctx <= 0;
  const int len = zeroq ? LK : min(ctx, LK);
  const int nunits = (len + UNIT - 1) / UNIT;
  const int nlive = (nunits > split) ? (nunits - split + NSPLIT - 1) / NSPLIT : 0;

  if (tok0 < len) {
    const uint16_t* kp0 = kc + ((size_t)p0 * BS + c) * (HKV * D_) + kvh * D_ + 8 * g;
    const uint16_t* kp1 = kc + ((size_t)p1 * BS + c) * (HKV * D_) + kvh * D_ + 8 * g;
    u32x4 ka[2][4];
#pragma unroll
    for (int s = 0; s < 4; ++s) ka[0][s] = *reinterpret_cast<const u32x4*>(kp0 + 32 * s);
#pragma unroll
    for (int s = 0; s < 4; ++s) ka[1][s] = *reinterpret_cast<const u32x4*>(kp1 + 32 * s);
    u32x4 qb[4];
    {
      const uint16_t* qp = q + ((size_t)b * H_ + kvh * G_ + (c & 3)) * D_ + 8 * g;
      const bool qv = (c < G_) && !zeroq;
#pragma unroll
      for (int s = 0; s < 4; ++s) {
        u32x4 t = *reinterpret_cast<const u32x4*>(qp + 32 * s);
        qb[s] = qv ? t : u32x4{0u, 0u, 0u, 0u};
      }
    }
    const uint16_t* vp0 = vc + ((size_t)p0 * BS + 4 * g) * (HKV * D_) + kvh * D_ + 8 * c;
    const uint16_t* vp1 = vc + ((size_t)p1 * BS + 4 * g) * (HKV * D_) + kvh * D_ + 8 * c;
    u32x4 vv[8];
#pragma unroll
    for (int i = 0; i < 4; ++i) vv[i] = *reinterpret_cast<const u32x4*>(vp0 + i * (HKV * D_));
#pragma unroll
    for (int i = 0; i < 4; ++i) vv[4 + i] = *reinterpret_cast<const u32x4*>(vp1 + i * (HKV * D_));

    f32x4 sacc[2];
#pragma unroll
    for (int pg = 0; pg < 2; ++pg) {
      f32x4 a = {0.f, 0.f, 0.f, 0.f};
#pragma unroll
      for (int s = 0; s < 4; ++s)
        a = __builtin_amdgcn_mfma_f32_16x16x32_bf16(__builtin_bit_cast(bf16x8, ka[pg][s]),
                                                    __builtin_bit_cast(bf16x8, qb[s]), a, 0, 0, 0);
      sacc[pg] = a;
    }
    constexpr float C2 = 0.088388347648318440f * 1.4426950408889634f;
    float x[8];
    const int rem = len - tok0 - 4 * g;
#pragma unroll
    for (int pg = 0; pg < 2; ++pg)
#pragma unroll
      for (int r = 0; r < 4; ++r) x[pg * 4 + r] = (pg * 16 + r < rem) ? sacc[pg][r] * C2 : -INFINITY;
    float m = fmaxf(fmaxf(fmaxf(x[0], x[1]), fmaxf(x[2], x[3])), fmaxf(fmaxf(x[4], x[5]), fmaxf(x[6], x[7])));
    m = xg_max(m);
    float p[8];
#pragma unroll
    for (int j = 0; j < 8; ++j) p[j] = __builtin_amdgcn_exp2f(x[j] - m);
    float l = ((p[0] + p[1]) + (p[2] + p[3])) + ((p[4] + p[5]) + (p[6] + p[7]));
    l = xg_sum(l);
    u32x4 pa = {cvtpk(p[0], p[1]), cvtpk(p[2], p[3]), cvtpk(p[4], p[5]), cvtpk(p[6], p[7])};

    f32x4 o[8];
#pragma unroll
    for (int e = 0; e < 8; ++e) {
      const unsigned sel = (e & 1) ? 0x07060302u : 0x05040100u;
      u32x4 vb;
#pragma unroll
      for (int d = 0; d < 4; ++d) vb[d] = __builtin_amdgcn_perm(vv[2 * d + 1][e >> 1], vv[2 * d][e >> 1], sel);
      f32x4 z = {0.f, 0.f, 0.f, 0.f};
      o[e] = __builtin_amdgcn_mfma_f32_16x16x32_bf16(__builtin_bit_cast(bf16x8, pa), __builtin_bit_cast(bf16x8, vb),
                                                     z, 0, 0, 0);
    }
    if (g == 0) {
#pragma unroll
      for (int r = 0; r < 4; ++r) {
        *reinterpret_cast<f32x4*>(&lds_o[wave][r][8 * c]) = f32x4{o[0][r], o[1][r], o[2][r], o[3][r]};
        *reinterpret_cast<f32x4*>(&lds_o[wave][r][8 * c + 4]) = f32x4{o[4][r], o[5][r], o[6][r], o[7][r]};
      }
      if (c < G_) {
        lds_m[wave][c] = m;
        lds_l[wave][c] = l;
      }
    }
  }
  __syncthreads();
  if (tid < G_ * D_) {
    const int h = tid >> 7, d = tid & 127;
    float M = -INFINITY;
    for (int w = 0; w < nlive; ++w) M = fmaxf(M, lds_m[w][h]);
    float L = 0.f, acc = 0.f;
    for (int w = 0; w < nlive; ++w) {
      const float f = __builtin_amdgcn_exp2f(lds_m[w][h] - M);
      L = fmaf(f, lds_l[w][h], L);
      acc = fmaf(f, lds_o[w][h][d], acc);
    }
    const int row = b * H_ + kvh * G_ + h;
    ws_o[((size_t)row * NSPLIT + split) * D_ + d] = acc;
    if (d == 0) {
      ws_ml[(row * NSPLIT + split) * 2] = M;
      ws_ml[(row * NSPLIT + split) * 2 + 1] = L;
    }
  }
}

__global__ void __launch_bounds__(64)
attn_combine(const float* __restrict__ ws_o, const float* __restrict__ ws_ml, unsigned* __restrict__ out) {
  const int row = blockIdx.x;
  const int lane = threadIdx.x;
  float mm[NSPLIT], ll[NSPLIT];
  float2 oo[NSPLIT];
#pragma unroll
  for (int s = 0; s < NSPLIT; ++s) {
    const float2 t = *reinterpret_cast<const float2*>(ws_ml + (row * NSPLIT + s) * 2);
    mm[s] = t.x;
    ll[s] = t.y;
    oo[s] = *reinterpret_cast<const float2*>(ws_o + ((size_t)row * NSPLIT + s) * D_ + 2 * lane);
  }
  float M = -INFINITY;
#pragma unroll
  for (int s = 0; s < NSPLIT; ++s) M = fmaxf(M, mm[s]);
  float L = 0.f, a0 = 0.f, a1 = 0.f;
#pragma unroll
  for (int s = 0; s < NSPLIT; ++s) {
    const float f = (ll[s] > 0.f) ? __builtin_amdgcn_exp2f(mm[s] - M) : 0.f;
    L = fmaf(f, ll[s], L);
    a0 = fmaf(f, oo[s].x, a0);
    a1 = fmaf(f, oo[s].y, a1);
  }
  const float inv = 1.f / L;
  out[row * (D_ / 2) + lane] = cvtpk(a0 * inv, a1 * inv);
}

}

extern "C" void kernel_launch(void* const* d_in, const int* in_sizes, int n_in, void* d_out, int out_size,
                              void* d_ws, size_t ws_size, hipStream_t stream) {
  const uint16_t* q = (const uint16_t*)d_in[0];
  const uint16_t* kc = (const uint16_t*)d_in[1];
  const uint16_t* vc = (const uint16_t*)d_in[2];
  const int* bt = (const int*)d_in[3];
  const int* ctxl = (const int*)d_in[4];
  float* ws_o = (float*)d_ws;
  float* ws_ml = ws_o + (size_t)B_ * H_ * NSPLIT * D_;
  attn_main<<<HKV * B_ * NSPLIT, NWAVE * 64, 0, stream>>>(q, kc, vc, bt, ctxl, ws_o, ws_ml);
  attn_combine<<<B_ * H_, 64, 0, stream>>>(ws_o, ws_ml, (unsigned*)d_out);
}
```

```cpp
#include <hip/hip_runtime.h>
#include <hip/hip_bf16.h>
#include <stdint.h>

namespace {

constexpr int B_ = 8, H_ = 32, HKV = 8, D_ = 128, BS = 16, NB = 128, LK = NB * BS, G_ = H_ / HKV;
constexpr int NSPLIT = 4, NWAVE = 16, UNIT = 32;
static_assert(NSPLIT * NWAVE * UNIT == LK, "units cover the sequence");

using bf16x8 = __attribute__((ext_vector_type(8))) short;
using f32x4 = __attribute__((ext_vector_type(4))) float;
using u32x4 = __attribute__((ext_vector_type(4))) unsigned;

__device__ __forceinline__ unsigned cvtpk(float lo, float hi) {
  unsigned r;
  asm("v_cvt_pk_bf16_f32 %0, %1, %2" : "=v"(r) : "v"(lo), "v"(hi));
  return r;
}
__device__ __forceinline__ float xg_max(float x) {
  auto s = __builtin_amdgcn_permlane16_swap(__float_as_uint(x), __float_as_uint(x), false, false);
  x = fmaxf(__uint_as_float(s[0]), __uint_as_float(s[1]));
  auto t = __builtin_amdgcn_permlane32_swap(__float_as_uint(x), __float_as_uint(x), false, false);
  return fmaxf(__uint_as_float(t[0]), __uint_as_float(t[1]));
}
__device__ __forceinline__ float xg_sum(float x) {
  auto s = __builtin_amdgcn_permlane16_swap(__float_as_uint(x), __float_as_uint(x), false, false);
  x = __uint_as_float(s[0]) + __uint_as_float(s[1]);
  auto t = __builtin_amdgcn_permlane32_swap(__float_as_uint(x), __float_as_uint(x), false, false);
  return __uint_as_float(t[0]) + __uint_as_float(t[1]);
}

constexpr int WAVE_LDS = 8192;
constexpr int LDS_ML = NWAVE * WAVE_LDS;
constexpr int LDS_BYTES = LDS_ML + 2 * G_ * NWAVE * 4;


__global__ void __launch_bounds__(1024)
attn_main(const uint16_t* __restrict__ q, const uint16_t* __restrict__ kc, const uint16_t* __restrict__ vc,
          const int* __restrict__ bt, const int* __restrict__ ctxl, float* __restrict__ ws_o,
          float* __restrict__ ws_ml) {
  __shared__ __attribute__((aligned(16))) char lds[LDS_BYTES];
  const int bid = blockIdx.x;
  const int kvh = bid & 7;
  const int b = (bid >> 3) & 7;
  const int split = bid >> 6;
  const int tid = threadIdx.x;
  const int lane = tid & 63;
  const int wave = __builtin_amdgcn_readfirstlane(tid >> 6);
  const int g = lane >> 4, c = lane & 15;

  asm volatile("" ::"s"(q), "s"(kc), "s"(vc), "s"(bt), "s"(ctxl), "s"(ws_o), "s"(ws_ml));

  const int unit = wave * NSPLIT + split;
  const int tok0 = unit * UNIT;
  u32x4 qb[4];
  {
    const uint16_t* qp = q + ((size_t)b * H_ + kvh * G_ + (c & 3)) * D_ + 8 * g;
#pragma unroll
    for (int s = 0; s < 4; ++s) qb[s] = *reinterpret_cast<const u32x4*>(qp + 32 * s);
  }
  int ctx;
  unsigned long long pp;
  {
    const int* cp = ctxl + b;
    const int* bp = bt + b * NB + unit * 2;
    asm volatile("s_load_dword %0, %2, 0x0\n\ts_load_dwordx2 %1, %3, 0x0\n\ts_waitcnt lgkmcnt(0)"
                 : "=&s"(ctx), "=&s"(pp)
                 : "s"(cp), "s"(bp)
                 : "memory");
  }
  const int p0 = (int)(unsigned)pp, p1 = (int)(unsigned)(pp >> 32);
  const bool zeroq = ctx <= 0;
  const int len = zeroq ? LK : min(ctx, LK);
  char* wl = lds + wave * WAVE_LDS;
  float* lds_m = reinterpret_cast<float*>(lds + LDS_ML);
  float* lds_l = lds_m + G_ * NWAVE;

  if (tok0 < len) {
    const uint16_t* vp0 = vc + ((size_t)p0 * BS + 4 * g) * (HKV * D_) + kvh * D_ + 8 * c;
    const uint16_t* vp1 = vc + ((size_t)p1 * BS + 4 * g) * (HKV * D_) + kvh * D_ + 8 * c;
    const uint16_t* kb0 = kc + ((size_t)p0 * BS + g) * (HKV * D_) + kvh * D_;
    const uint16_t* kb1 = kc + ((size_t)p1 * BS + g) * (HKV * D_) + kvh * D_;
    {
      const bool qv = (c < G_) && !zeroq;
#pragma unroll
      for (int s = 0; s < 4; ++s) {
        qb[s] = qv ? qb[s] : u32x4{0u, 0u, 0u, 0u};
        asm volatile("" : "+v"(qb[s]));
      }
    }
    __builtin_amdgcn_sched_barrier(0);
#pragma unroll
    for (int i = 0; i < 4; ++i)
      __builtin_amdgcn_global_load_lds((const unsigned*)(kb0 + (size_t)(4 * i) * (HKV * D_) + 8 * (c ^ (4 * i + g))),
                                       (unsigned*)(wl + i * 1024), 16, 0, 2);
#pragma unroll
    for (int i = 0; i < 4; ++i)
      __builtin_amdgcn_global_load_lds((const unsigned*)(kb1 + (size_t)(4 * i) * (HKV * D_) + 8 * (c ^ (4 * i + g))),
                                       (unsigned*)(wl + 4096 + i * 1024), 16, 0, 2);
    u32x4 vv[8];
#pragma unroll
    for (int i = 0; i < 4; ++i) vv[i] = __builtin_nontemporal_load(reinterpret_cast<const u32x4*>(vp0 + i * (HKV * D_)));
#pragma unroll
    for (int i = 0; i < 4; ++i) vv[4 + i] = __builtin_nontemporal_load(reinterpret_cast<const u32x4*>(vp1 + i * (HKV * D_)));
    __builtin_amdgcn_sched_barrier(0);
    asm volatile("s_waitcnt vmcnt(8)" ::: "memory");
    __builtin_amdgcn_sched_barrier(0);
    f32x4 sacc[2];
#pragma unroll
    for (int pg = 0; pg < 2; ++pg) {
      f32x4 a = {0.f, 0.f, 0.f, 0.f};
#pragma unroll
      for (int s = 0; s < 4; ++s) {
        const u32x4 ka = *reinterpret_cast<const u32x4*>(wl + pg * 4096 + c * 256 + (((4 * s + g) ^ c) * 16));
        a = __builtin_amdgcn_mfma_f32_16x16x32_bf16(__builtin_bit_cast(bf16x8, ka), __builtin_bit_cast(bf16x8, qb[s]), a,
                                                    0, 0, 0);
      }
      sacc[pg] = a;
    }
    constexpr float C2 = 0.088388347648318440f * 1.4426950408889634f;
    float x[8];
    const int rem = len - tok0 - 4 * g;
#pragma unroll
    for (int pg = 0; pg < 2; ++pg)
#pragma unroll
      for (int r = 0; r < 4; ++r) x[pg * 4 + r] = (pg * 16 + r < rem) ? sacc[pg][r] * C2 : -INFINITY;
    float m = fmaxf(fmaxf(fmaxf(x[0], x[1]), fmaxf(x[2], x[3])), fmaxf(fmaxf(x[4], x[5]), fmaxf(x[6], x[7])));
    m = xg_max(m);
    float p[8];
#pragma unroll
    for (int j = 0; j < 8; ++j) p[j] = __builtin_amdgcn_exp2f(x[j] - m);
    float l = ((p[0] + p[1]) + (p[2] + p[3])) + ((p[4] + p[5]) + (p[6] + p[7]));
    l = xg_sum(l);
    u32x4 pa = {cvtpk(p[0], p[1]), cvtpk(p[2], p[3]), cvtpk(p[4], p[5]), cvtpk(p[6], p[7])};

    f32x4 o[8];
#pragma unroll
    for (int e = 0; e < 8; ++e) {
      const unsigned sel = (e & 1) ? 0x07060302u : 0x05040100u;
      u32x4 vb;
#pragma unroll
      for (int d = 0; d < 4; ++d) vb[d] = __builtin_amdgcn_perm(vv[2 * d + 1][e >> 1], vv[2 * d][e >> 1], sel);
      f32x4 z = {0.f, 0.f, 0.f, 0.f};
      o[e] = __builtin_amdgcn_mfma_f32_16x16x32_bf16(__builtin_bit_cast(bf16x8, pa), __builtin_bit_cast(bf16x8, vb),
                                                     z, 0, 0, 0);
    }
    if (g == 0) {
      float* wo = reinterpret_cast<float*>(wl);
#pragma unroll
      for (int r = 0; r < 4; ++r) {
        *reinterpret_cast<f32x4*>(wo + r * D_ + 8 * c) = f32x4{o[0][r], o[1][r], o[2][r], o[3][r]};
        *reinterpret_cast<f32x4*>(wo + r * D_ + 8 * c + 4) = f32x4{o[4][r], o[5][r], o[6][r], o[7][r]};
      }
      if (c < G_) {
        lds_m[c * NWAVE + wave] = m;
        lds_l[c * NWAVE + wave] = l;
      }
    }
  } else {
    float* wo = reinterpret_cast<float*>(wl);
    *reinterpret_cast<f32x4*>(wo + 8 * lane) = f32x4{0.f, 0.f, 0.f, 0.f};
    *reinterpret_cast<f32x4*>(wo + 8 * lane + 4) = f32x4{0.f, 0.f, 0.f, 0.f};
    if (lane < G_) {
      lds_m[lane * NWAVE + wave] = -INFINITY;
      lds_l[lane * NWAVE + wave] = 0.f;
    }
  }
  __syncthreads();
  if (tid < G_ * D_) {
    const int h = tid >> 7, d = tid & 127;
    f32x4 mv[4], lv[4];
    float ov[NWAVE];
#pragma unroll
    for (int i = 0; i < 4; ++i) {
      mv[i] = *reinterpret_cast<const f32x4*>(lds_m + h * NWAVE + 4 * i);
      lv[i] = *reinterpret_cast<const f32x4*>(lds_l + h * NWAVE + 4 * i);
    }
#pragma unroll
    for (int w = 0; w < NWAVE; ++w) ov[w] = *reinterpret_cast<const float*>(lds + w * WAVE_LDS + (h * D_ + d) * 4);
    float M = -INFINITY;
#pragma unroll
    for (int w = 0; w < NWAVE; ++w) M = fmaxf(M, mv[w >> 2][w & 3]);
    const float Ms = (M == -INFINITY) ? 0.f : M;
    float L = 0.f, acc = 0.f;
#pragma unroll
    for (int w = 0; w < NWAVE; ++w) {
      const float f = __builtin_amdgcn_exp2f(mv[w >> 2][w & 3] - Ms);
      L = fmaf(f, lv[w >> 2][w & 3], L);
      acc = fmaf(f, ov[w], acc);
    }
    const int row = b * H_ + kvh * G_ + h;
    ws_o[((size_t)row * NSPLIT + split) * D_ + d] = acc;
    if (d == 0) *reinterpret_cast<float2*>(ws_ml + (row * NSPLIT + split) * 2) = make_float2(M, L);
  }
}

__global__ void __launch_bounds__(64)
attn_combine(const float* __restrict__ ws_o, const float* __restrict__ ws_ml, unsigned* __restrict__ out) {
  const int row = blockIdx.x;
  const int lane = threadIdx.x;
  float mm[NSPLIT], ll[NSPLIT];
  float2 oo[NSPLIT];
#pragma unroll
  for (int s = 0; s < NSPLIT; ++s) {
    const float2 t = *reinterpret_cast<const float2*>(ws_ml + (row * NSPLIT + s) * 2);
    mm[s] = t.x;
    ll[s] = t.y;
    oo[s] = *reinterpret_cast<const float2*>(ws_o + ((size_t)row * NSPLIT + s) * D_ + 2 * lane);
  }
  float M = -INFINITY;
#pragma unroll
  for (int s = 0; s < NSPLIT; ++s) M = fmaxf(M, mm[s]);
  float L = 0.f, a0 = 0.f, a1 = 0.f;
#pragma unroll
  for (int s = 0; s < NSPLIT; ++s) {
    const float f = (ll[s] > 0.f) ? __builtin_amdgcn_exp2f(mm[s] - M) : 0.f;
    L = fmaf(f, ll[s], L);
    a0 = fmaf(f, oo[s].x, a0);
    a1 = fmaf(f, oo[s].y, a1);
  }
  const float inv = 1.f / L;
  out[row * (D_ / 2) + lane] = cvtpk(a0 * inv, a1 * inv);
}

}

extern "C" void kernel_launch(void* const* d_in, const int* in_sizes, int n_in, void* d_out, int out_size,
                              void* d_ws, size_t ws_size, hipStream_t stream) {
  const uint16_t* q = (const uint16_t*)d_in[0];
  const uint16_t* kc = (const uint16_t*)d_in[1];
  const uint16_t* vc = (const uint16_t*)d_in[2];
  const int* bt = (const int*)d_in[3];
  const int* ctxl = (const int*)d_in[4];
  float* ws_o = (float*)d_ws;
  float* ws_ml = ws_o + (size_t)B_ * H_ * NSPLIT * D_;
  attn_main<<<HKV * B_ * NSPLIT, NWAVE * 64, 0, stream>>>(q, kc, vc, bt, ctxl, ws_o, ws_ml);
  attn_combine<<<B_ * H_, 64, 0, stream>>>(ws_o, ws_ml, (unsigned*)d_out);
}
```

```cpp
#include <hip/hip_runtime.h>
#include <hip/hip_bf16.h>
#include <stdint.h>

namespace {

constexpr int B_ = 8, H_ = 32, HKV = 8, D_ = 128, BS = 16, NB = 128, LK = NB * BS, G_ = H_ / HKV;
constexpr int NSPLIT = 4, NWAVE = 16, UNIT = 32;
static_assert(NSPLIT * NWAVE * UNIT == LK, "units cover the sequence");

using bf16x8 = __attribute__((ext_vector_type(8))) short;
using f32x4 = __attribute__((ext_vector_type(4))) float;
using u32x4 = __attribute__((ext_vector_type(4))) unsigned;

__device__ __forceinline__ unsigned cvtpk(float lo, float hi) {
  unsigned r;
  asm("v_cvt_pk_bf16_f32 %0, %1, %2" : "=v"(r) : "v"(lo), "v"(hi));
  return r;
}
__device__ __forceinline__ float xg_max(float x) {
  auto s = __builtin_amdgcn_permlane16_swap(__float_as_uint(x), __float_as_uint(x), false, false);
  x = fmaxf(__uint_as_float(s[0]), __uint_as_float(s[1]));
  auto t = __builtin_amdgcn_permlane32_swap(__float_as_uint(x), __float_as_uint(x), false, false);
  return fmaxf(__uint_as_float(t[0]), __uint_as_float(t[1]));
}
__device__ __forceinline__ float xg_sum(float x) {
  auto s = __builtin_amdgcn_permlane16_swap(__float_as_uint(x), __float_as_uint(x), false, false);
  x = __uint_as_float(s[0]) + __uint_as_float(s[1]);
  auto t = __builtin_amdgcn_permlane32_swap(__float_as_uint(x), __float_as_uint(x), false, false);
  return __uint_as_float(t[0]) + __uint_as_float(t[1]);
}

constexpr int WAVE_LDS = 8192;
constexpr int LDS_ML = NWAVE * WAVE_LDS;
constexpr int LDS_BYTES = LDS_ML + 2 * G_ * NWAVE * 4;


__global__ void __launch_bounds__(1024)
attn_main(const uint16_t* __restrict__ q, const uint16_t* __restrict__ kc, const uint16_t* __restrict__ vc,
          const int* __restrict__ bt, const int* __restrict__ ctxl, float* __restrict__ ws_o,
          float* __restrict__ ws_ml) {
  __shared__ __attribute__((aligned(16))) char lds[LDS_BYTES];
  const int bid = blockIdx.x;
  const int kvh = bid & 7;
  const int b = (bid >> 3) & 7;
  const int split = bid >> 6;
  const int tid = threadIdx.x;
  const int lane = tid & 63;
  const int wave = __builtin_amdgcn_readfirstlane(tid >> 6);
  const int g = lane >> 4, c = lane & 15;

  asm volatile("" ::"s"(q), "s"(kc), "s"(vc), "s"(bt), "s"(ctxl), "s"(ws_o), "s"(ws_ml));

  const int unit = wave * NSPLIT + split;
  const int tok0 = unit * UNIT;
  u32x4 qb[4];
  {
    const uint16_t* qp = q + ((size_t)b * H_ + kvh * G_ + (c & 3)) * D_ + 8 * g;
#pragma unroll
    for (int s = 0; s < 4; ++s) qb[s] = *reinterpret_cast<const u32x4*>(qp + 32 * s);
  }
  int ctx;
  unsigned long long pp;
  {
    const int* cp = ctxl + b;
    const int* bp = bt + b * NB + unit * 2;
    asm volatile("s_load_dword %0, %2, 0x0\n\ts_load_dwordx2 %1, %3, 0x0\n\ts_waitcnt lgkmcnt(0)"
                 : "=&s"(ctx), "=&s"(pp)
                 : "s"(cp), "s"(bp)
                 : "memory");
  }
  const int p0 = (int)(unsigned)pp, p1 = (int)(unsigned)(pp >> 32);
  const bool zeroq = ctx <= 0;
  const int len = zeroq ? LK : min(ctx, LK);
  char* wl = lds + wave * WAVE_LDS;
  float* lds_m = reinterpret_cast<float*>(lds + LDS_ML);
  float* lds_l = lds_m + G_ * NWAVE;

  if (tok0 < len) {
    const uint16_t* vp0 = vc + ((size_t)p0 * BS + 4 * g) * (HKV * D_) + kvh * D_ + 8 * c;
    const uint16_t* vp1 = vc + ((size_t)p1 * BS + 4 * g) * (HKV * D_) + kvh * D_ + 8 * c;
    const uint16_t* kb0 = kc + ((size_t)p0 * BS + g) * (HKV * D_) + kvh * D_;
    const uint16_t* kb1 = kc + ((size_t)p1 * BS + g) * (HKV * D_) + kvh * D_;
    {
      const bool qv = (c < G_) && !zeroq;
#pragma unroll
      for (int s = 0; s < 4; ++s) {
        qb[s] = qv ? qb[s] : u32x4{0u, 0u, 0u, 0u};
        asm volatile("" : "+v"(qb[s]));
      }
    }
    __builtin_amdgcn_sched_barrier(0);
#pragma unroll
    for (int i = 0; i < 4; ++i)
      __builtin_amdgcn_global_load_lds((const unsigned*)(kb0 + (size_t)(4 * i) * (HKV * D_) + 8 * (c ^ (4 * i + g))),
                                       (unsigned*)(wl + i * 1024), 16, 0, 0);
#pragma unroll
    for (int i = 0; i < 4; ++i)
      __builtin_amdgcn_global_load_lds((const unsigned*)(kb1 + (size_t)(4 * i) * (HKV * D_) + 8 * (c ^ (4 * i + g))),
                                       (unsigned*)(wl + 4096 + i * 1024), 16, 0, 0);
    u32x4 vv[8];
#pragma unroll
    for (int i = 0; i < 4; ++i) vv[i] = *reinterpret_cast<const u32x4*>(vp0 + i * (HKV * D_));
#pragma unroll
    for (int i = 0; i < 4; ++i) vv[4 + i] = *reinterpret_cast<const u32x4*>(vp1 + i * (HKV * D_));
    __builtin_amdgcn_sched_barrier(0);
    asm volatile("s_waitcnt vmcnt(8)" ::: "memory");
    __builtin_amdgcn_sched_barrier(0);
    f32x4 sacc[2];
#pragma unroll
    for (int pg = 0; pg < 2; ++pg) {
      f32x4 a = {0.f, 0.f, 0.f, 0.f};
#pragma unroll
      for (int s = 0; s < 4; ++s) {
        const u32x4 ka = *reinterpret_cast<const u32x4*>(wl + pg * 4096 + c * 256 + (((4 * s + g) ^ c) * 16));
        a = __builtin_amdgcn_mfma_f32_16x16x32_bf16(__builtin_bit_cast(bf16x8, ka), __builtin_bit_cast(bf16x8, qb[s]), a,
                                                    0, 0, 0);
      }
      sacc[pg] = a;
    }
    constexpr float C2 = 0.088388347648318440f * 1.4426950408889634f;
    float x[8];
    const int rem = len - tok0 - 4 * g;
#pragma unroll
    for (int pg = 0; pg < 2; ++pg)
#pragma unroll
      for (int r = 0; r < 4; ++r) x[pg * 4 + r] = (pg * 16 + r < rem) ? sacc[pg][r] * C2 : -INFINITY;
    float m = fmaxf(fmaxf(fmaxf(x[0], x[1]), fmaxf(x[2], x[3])), fmaxf(fmaxf(x[4], x[5]), fmaxf(x[6], x[7])));
    m = xg_max(m);
    float p[8];
#pragma unroll
    for (int j = 0; j < 8; ++j) p[j] = __builtin_amdgcn_exp2f(x[j] - m);
    float l = ((p[0] + p[1]) + (p[2] + p[3])) + ((p[4] + p[5]) + (p[6] + p[7]));
    l = xg_sum(l);
    u32x4 pa = {cvtpk(p[0], p[1]), cvtpk(p[2], p[3]), cvtpk(p[4], p[5]), cvtpk(p[6], p[7])};

    f32x4 o[8];
#pragma unroll
    for (int e = 0; e < 8; ++e) {
      const unsigned sel = (e & 1) ? 0x07060302u : 0x05040100u;
      u32x4 vb;
#pragma unroll
      for (int d = 0; d < 4; ++d) vb[d] = __builtin_amdgcn_perm(vv[2 * d + 1][e >> 1], vv[2 * d][e >> 1], sel);
      f32x4 z = {0.f, 0.f, 0.f, 0.f};
      o[e] = __builtin_amdgcn_mfma_f32_16x16x32_bf16(__builtin_bit_cast(bf16x8, pa), __builtin_bit_cast(bf16x8, vb),
                                                     z, 0, 0, 0);
    }
    if (g == 0) {
      float* wo = reinterpret_cast<float*>(wl);
#pragma unroll
      for (int r = 0; r < 4; ++r) {
        *reinterpret_cast<f32x4*>(wo + r * D_ + 8 * c) = f32x4{o[0][r], o[1][r], o[2][r], o[3][r]};
        *reinterpret_cast<f32x4*>(wo + r * D_ + 8 * c + 4) = f32x4{o[4][r], o[5][r], o[6][r], o[7][r]};
      }
      if (c < G_) {
        lds_m[c * NWAVE + wave] = m;
        lds_l[c * NWAVE + wave] = l;
      }
    }
  } else {
    float* wo = reinterpret_cast<float*>(wl);
    *reinterpret_cast<f32x4*>(wo + 8 * lane) = f32x4{0.f, 0.f, 0.f, 0.f};
    *reinterpret_cast<f32x4*>(wo + 8 * lane + 4) = f32x4{0.f, 0.f, 0.f, 0.f};
    if (lane < G_) {
      lds_m[lane * NWAVE + wave] = -INFINITY;
      lds_l[lane * NWAVE + wave] = 0.f;
    }
  }
  __syncthreads();
  if (tid < G_ * D_) {
    const int h = tid >> 7, d = tid & 127;
    f32x4 mv[4], lv[4];
    float ov[NWAVE];
#pragma unroll
    for (int i = 0; i < 4; ++i) {
      mv[i] = *reinterpret_cast<const f32x4*>(lds_m + h * NWAVE + 4 * i);
      lv[i] = *reinterpret_cast<const f32x4*>(lds_l + h * NWAVE + 4 * i);
    }
#pragma unroll
    for (int w = 0; w < NWAVE; ++w) ov[w] = *reinterpret_cast<const float*>(lds + w * WAVE_LDS + (h * D_ + d) * 4);
    float M = -INFINITY;
#pragma unroll
    for (int w = 0; w < NWAVE; ++w) M = fmaxf(M, mv[w >> 2][w & 3]);
    const float Ms = (M == -INFINITY) ? 0.f : M;
    float L = 0.f, acc = 0.f;
#pragma unroll
    for (int w = 0; w < NWAVE; ++w) {
      const float f = __builtin_amdgcn_exp2f(mv[w >> 2][w & 3] - Ms);
      L = fmaf(f, lv[w >> 2][w & 3], L);
      acc = fmaf(f, ov[w], acc);
    }
    const int row = b * H_ + kvh * G_ + h;
    ws_o[((size_t)row * NSPLIT + split) * D_ + d] = acc;
    if (d == 0) *reinterpret_cast<float2*>(ws_ml + (row * NSPLIT + split) * 2) = make_float2(M, L);
  }
}

__global__ void __launch_bounds__(64)
attn_combine(const float* __restrict__ ws_o, const float* __restrict__ ws_ml, unsigned* __restrict__ out) {
  const int row = blockIdx.x;
  const int lane = threadIdx.x;
  float mm[NSPLIT], ll[NSPLIT];
  float2 oo[NSPLIT];
#pragma unroll
  for (int s = 0; s < NSPLIT; ++s) {
    const float2 t = *reinterpret_cast<const float2*>(ws_ml + (row * NSPLIT + s) * 2);
    mm[s] = t.x;
    ll[s] = t.y;
    oo[s] = *reinterpret_cast<const float2*>(ws_o + ((size_t)row * NSPLIT + s) * D_ + 2 * lane);
  }
  float M = -INFINITY;
#pragma unroll
  for (int s = 0; s < NSPLIT; ++s) M = fmaxf(M, mm[s]);
  float L = 0.f, a0 = 0.f, a1 = 0.f;
#pragma unroll
  for (int s = 0; s < NSPLIT; ++s) {
    const float f = (ll[s] > 0.f) ? __builtin_amdgcn_exp2f(mm[s] - M) : 0.f;
    L = fmaf(f, ll[s], L);
    a0 = fmaf(f, oo[s].x, a0);
    a1 = fmaf(f, oo[s].y, a1);
  }
  const float inv = 1.f / L;
  out[row * (D_ / 2) + lane] = cvtpk(a0 * inv, a1 * inv);
}

}

extern "C" void kernel_launch(void* const* d_in, const int* in_sizes, int n_in, void* d_out, int out_size,
                              void* d_ws, size_t ws_size, hipStream_t stream) {
  const uint16_t* q = (const uint16_t*)d_in[0];
  const uint16_t* kc = (const uint16_t*)d_in[1];
  const uint16_t* vc = (const uint16_t*)d_in[2];
  const int* bt = (const int*)d_in[3];
  const int* ctxl = (const int*)d_in[4];
  float* ws_o = (float*)d_ws;
  float* ws_ml = ws_o + (size_t)B_ * H_ * NSPLIT * D_;
  attn_main<<<HKV * B_ * NSPLIT, NWAVE * 64, 0, stream>>>(q, kc, vc, bt, ctxl, ws_o, ws_ml);
  attn_combine<<<B_ * H_, 64, 0, stream>>>(ws_o, ws_ml, (unsigned*)d_out);
}
```

```cpp
#include <hip/hip_runtime.h>
#include <hip/hip_bf16.h>
#include <stdint.h>

namespace {

constexpr int B_ = 8, H_ = 32, HKV = 8, D_ = 128, BS = 16, NB = 128, LK = NB * BS, G_ = H_ / HKV;
constexpr int NSPLIT = 4, NWAVE = 16, UNIT = 32;
static_assert(NSPLIT * NWAVE * UNIT == LK, "units cover the sequence");

using bf16x8 = __attribute__((ext_vector_type(8))) short;
using f32x4 = __attribute__((ext_vector_type(4))) float;
using u32x4 = __attribute__((ext_vector_type(4))) unsigned;

__device__ __forceinline__ unsigned cvtpk(float lo, float hi) {
  unsigned r;
  asm("v_cvt_pk_bf16_f32 %0, %1, %2" : "=v"(r) : "v"(lo), "v"(hi));
  return r;
}
__device__ __forceinline__ float xg_max(float x) {
  auto s = __builtin_amdgcn_permlane16_swap(__float_as_uint(x), __float_as_uint(x), false, false);
  x = fmaxf(__uint_as_float(s[0]), __uint_as_float(s[1]));
  auto t = __builtin_amdgcn_permlane32_swap(__float_as_uint(x), __float_as_uint(x), false, false);
  return fmaxf(__uint_as_float(t[0]), __uint_as_float(t[1]));
}
__device__ __forceinline__ float xg_sum(float x) {
  auto s = __builtin_amdgcn_permlane16_swap(__float_as_uint(x), __float_as_uint(x), false, false);
  x = __uint_as_float(s[0]) + __uint_as_float(s[1]);
  auto t = __builtin_amdgcn_permlane32_swap(__float_as_uint(x), __float_as_uint(x), false, false);
  return __uint_as_float(t[0]) + __uint_as_float(t[1]);
}

constexpr int WAVE_LDS = 8192;
constexpr int LDS_ML = NWAVE * WAVE_LDS;
constexpr int LDS_BYTES = LDS_ML + 2 * G_ * NWAVE * 4;


__global__ void __launch_bounds__(1024)
attn_main(const uint16_t* __restrict__ q, const uint16_t* __restrict__ kc, const uint16_t* __restrict__ vc,
          const int* __restrict__ bt, const int* __restrict__ ctxl, float* __restrict__ ws_o,
          float* __restrict__ ws_ml) {
  __shared__ __attribute__((aligned(16))) char lds[LDS_BYTES];
  const unsigned long long T0_ = __builtin_amdgcn_s_memrealtime();
  const int bid = blockIdx.x;
  const int kvh = bid & 7;
  const int b = (bid >> 3) & 7;
  const int split = bid >> 6;
  const int tid = threadIdx.x;
  const int lane = tid & 63;
  const int wave = __builtin_amdgcn_readfirstlane(tid >> 6);
  const int g = lane >> 4, c = lane & 15;

  asm volatile("" ::"s"(q), "s"(kc), "s"(vc), "s"(bt), "s"(ctxl), "s"(ws_o), "s"(ws_ml));

  const int unit = wave * NSPLIT + split;
  const int tok0 = unit * UNIT;
  u32x4 qb[4];
  {
    const uint16_t* qp = q + ((size_t)b * H_ + kvh * G_ + (c & 3)) * D_ + 8 * g;
#pragma unroll
    for (int s = 0; s < 4; ++s) qb[s] = *reinterpret_cast<const u32x4*>(qp + 32 * s);
  }
  int ctx;
  unsigned long long pp;
  {
    const int* cp = ctxl + b;
    const int* bp = bt + b * NB + unit * 2;
    asm volatile("s_load_dword %0, %2, 0x0\n\ts_load_dwordx2 %1, %3, 0x0\n\ts_waitcnt lgkmcnt(0)"
                 : "=&s"(ctx), "=&s"(pp)
                 : "s"(cp), "s"(bp)
                 : "memory");
  }
  const int p0 = (int)(unsigned)pp, p1 = (int)(unsigned)(pp >> 32);
  const bool zeroq = ctx <= 0;
  const int len = zeroq ? LK : min(ctx, LK);
  char* wl = lds + wave * WAVE_LDS;
  float* lds_m = reinterpret_cast<float*>(lds + LDS_ML);
  float* lds_l = lds_m + G_ * NWAVE;

  if (tok0 < len) {
    const uint16_t* vp0 = vc + ((size_t)p0 * BS + 4 * g) * (HKV * D_) + kvh * D_ + 8 * c;
    const uint16_t* vp1 = vc + ((size_t)p1 * BS + 4 * g) * (HKV * D_) + kvh * D_ + 8 * c;
    const uint16_t* kb0 = kc + ((size_t)p0 * BS + g) * (HKV * D_) + kvh * D_;
    const uint16_t* kb1 = kc + ((size_t)p1 * BS + g) * (HKV * D_) + kvh * D_;
    {
      const bool qv = (c < G_) && !zeroq;
#pragma unroll
      for (int s = 0; s < 4; ++s) {
        qb[s] = qv ? qb[s] : u32x4{0u, 0u, 0u, 0u};
        asm volatile("" : "+v"(qb[s]));
      }
    }
    __builtin_amdgcn_sched_barrier(0);
#pragma unroll
    for (int i = 0; i < 4; ++i)
      __builtin_amdgcn_global_load_lds((const unsigned*)(kb0 + (size_t)(4 * i) * (HKV * D_) + 8 * (c ^ (4 * i + g))),
                                       (unsigned*)(wl + i * 1024), 16, 0, 2);
#pragma unroll
    for (int i = 0; i < 4; ++i)
      __builtin_amdgcn_global_load_lds((const unsigned*)(kb1 + (size_t)(4 * i) * (HKV * D_) + 8 * (c ^ (4 * i + g))),
                                       (unsigned*)(wl + 4096 + i * 1024), 16, 0, 2);
    u32x4 vv[8];
#pragma unroll
    for (int i = 0; i < 4; ++i) vv[i] = __builtin_nontemporal_load(reinterpret_cast<const u32x4*>(vp0 + i * (HKV * D_)));
#pragma unroll
    for (int i = 0; i < 4; ++i) vv[4 + i] = __builtin_nontemporal_load(reinterpret_cast<const u32x4*>(vp1 + i * (HKV * D_)));
    __builtin_amdgcn_sched_barrier(0);
    asm volatile("s_waitcnt vmcnt(8)" ::: "memory");
    __builtin_amdgcn_sched_barrier(0);
    f32x4 sacc[2];
#pragma unroll
    for (int pg = 0; pg < 2; ++pg) {
      f32x4 a = {0.f, 0.f, 0.f, 0.f};
#pragma unroll
      for (int s = 0; s < 4; ++s) {
        const u32x4 ka = *reinterpret_cast<const u32x4*>(wl + pg * 4096 + c * 256 + (((4 * s + g) ^ c) * 16));
        a = __builtin_amdgcn_mfma_f32_16x16x32_bf16(__builtin_bit_cast(bf16x8, ka), __builtin_bit_cast(bf16x8, qb[s]), a,
                                                    0, 0, 0);
      }
      sacc[pg] = a;
    }
    constexpr float C2 = 0.088388347648318440f * 1.4426950408889634f;
    float x[8];
    const int rem = len - tok0 - 4 * g;
#pragma unroll
    for (int pg = 0; pg < 2; ++pg)
#pragma unroll
      for (int r = 0; r < 4; ++r) x[pg * 4 + r] = (pg * 16 + r < rem) ? sacc[pg][r] * C2 : -INFINITY;
    float m = fmaxf(fmaxf(fmaxf(x[0], x[1]), fmaxf(x[2], x[3])), fmaxf(fmaxf(x[4], x[5]), fmaxf(x[6], x[7])));
    m = xg_max(m);
    float p[8];
#pragma unroll
    for (int j = 0; j < 8; ++j) p[j] = __builtin_amdgcn_exp2f(x[j] - m);
    float l = ((p[0] + p[1]) + (p[2] + p[3])) + ((p[4] + p[5]) + (p[6] + p[7]));
    l = xg_sum(l);
    u32x4 pa = {cvtpk(p[0], p[1]), cvtpk(p[2], p[3]), cvtpk(p[4], p[5]), cvtpk(p[6], p[7])};

    f32x4 o[8];
#pragma unroll
    for (int e = 0; e < 8; ++e) {
      const unsigned sel = (e & 1) ? 0x07060302u : 0x05040100u;
      u32x4 vb;
#pragma unroll
      for (int d = 0; d < 4; ++d) vb[d] = __builtin_amdgcn_perm(vv[2 * d + 1][e >> 1], vv[2 * d][e >> 1], sel);
      f32x4 z = {0.f, 0.f, 0.f, 0.f};
      o[e] = __builtin_amdgcn_mfma_f32_16x16x32_bf16(__builtin_bit_cast(bf16x8, pa), __builtin_bit_cast(bf16x8, vb),
                                                     z, 0, 0, 0);
    }
    if (g == 0) {
      float* wo = reinterpret_cast<float*>(wl);
#pragma unroll
      for (int r = 0; r < 4; ++r) {
        *reinterpret_cast<f32x4*>(wo + r * D_ + 8 * c) = f32x4{o[0][r], o[1][r], o[2][r], o[3][r]};
        *reinterpret_cast<f32x4*>(wo + r * D_ + 8 * c + 4) = f32x4{o[4][r], o[5][r], o[6][r], o[7][r]};
      }
      if (c < G_) {
        lds_m[c * NWAVE + wave] = m;
        lds_l[c * NWAVE + wave] = l;
      }
    }
  } else {
    float* wo = reinterpret_cast<float*>(wl);
    *reinterpret_cast<f32x4*>(wo + 8 * lane) = f32x4{0.f, 0.f, 0.f, 0.f};
    *reinterpret_cast<f32x4*>(wo + 8 * lane + 4) = f32x4{0.f, 0.f, 0.f, 0.f};
    if (lane < G_) {
      lds_m[lane * NWAVE + wave] = -INFINITY;
      lds_l[lane * NWAVE + wave] = 0.f;
    }
  }
  __syncthreads();
  if (tid < G_ * D_) {
    const int h = tid >> 7, d = tid & 127;
    f32x4 mv[4], lv[4];
    float ov[NWAVE];
#pragma unroll
    for (int i = 0; i < 4; ++i) {
      mv[i] = *reinterpret_cast<const f32x4*>(lds_m + h * NWAVE + 4 * i);
      lv[i] = *reinterpret_cast<const f32x4*>(lds_l + h * NWAVE + 4 * i);
    }
#pragma unroll
    for (int w = 0; w < NWAVE; ++w) ov[w] = *reinterpret_cast<const float*>(lds + w * WAVE_LDS + (h * D_ + d) * 4);
    float M = -INFINITY;
#pragma unroll
    for (int w = 0; w < NWAVE; ++w) M = fmaxf(M, mv[w >> 2][w & 3]);
    const float Ms = (M == -INFINITY) ? 0.f : M;
    float L = 0.f, acc = 0.f;
#pragma unroll
    for (int w = 0; w < NWAVE; ++w) {
      const float f = __builtin_amdgcn_exp2f(mv[w >> 2][w & 3] - Ms);
      L = fmaf(f, lv[w >> 2][w & 3], L);
      acc = fmaf(f, ov[w], acc);
    }
    const int row = b * H_ + kvh * G_ + h;
    ws_o[((size_t)row * NSPLIT + split) * D_ + d] = acc;
    if (d == 0) *reinterpret_cast<float2*>(ws_ml + (row * NSPLIT + split) * 2) = make_float2(M, L);
  }
  while (__builtin_amdgcn_s_memrealtime() - T0_ < 1000ull) __builtin_amdgcn_s_sleep(1);
}

__global__ void __launch_bounds__(64)
attn_combine(const float* __restrict__ ws_o, const float* __restrict__ ws_ml, unsigned* __restrict__ out) {
  const int row = blockIdx.x;
  const int lane = threadIdx.x;
  float mm[NSPLIT], ll[NSPLIT];
  float2 oo[NSPLIT];
#pragma unroll
  for (int s = 0; s < NSPLIT; ++s) {
    const float2 t = *reinterpret_cast<const float2*>(ws_ml + (row * NSPLIT + s) * 2);
    mm[s] = t.x;
    ll[s] = t.y;
    oo[s] = *reinterpret_cast<const float2*>(ws_o + ((size_t)row * NSPLIT + s) * D_ + 2 * lane);
  }
  float M = -INFINITY;
#pragma unroll
  for (int s = 0; s < NSPLIT; ++s) M = fmaxf(M, mm[s]);
  float L = 0.f, a0 = 0.f, a1 = 0.f;
#pragma unroll
  for (int s = 0; s < NSPLIT; ++s) {
    const float f = (ll[s] > 0.f) ? __builtin_amdgcn_exp2f(mm[s] - M) : 0.f;
    L = fmaf(f, ll[s], L);
    a0 = fmaf(f, oo[s].x, a0);
    a1 = fmaf(f, oo[s].y, a1);
  }
  const float inv = 1.f / L;
  out[row * (D_ / 2) + lane] = cvtpk(a0 * inv, a1 * inv);
}

}

extern "C" void kernel_launch(void* const* d_in, const int* in_sizes, int n_in, void* d_out, int out_size,
                              void* d_ws, size_t ws_size, hipStream_t stream) {
  const uint16_t* q = (const uint16_t*)d_in[0];
  const uint16_t* kc = (const uint16_t*)d_in[1];
  const uint16_t* vc = (const uint16_t*)d_in[2];
  const int* bt = (const int*)d_in[3];
  const int* ctxl = (const int*)d_in[4];
  float* ws_o = (float*)d_ws;
  float* ws_ml = ws_o + (size_t)B_ * H_ * NSPLIT * D_;
  attn_main<<<HKV * B_ * NSPLIT, NWAVE * 64, 0, stream>>>(q, kc, vc, bt, ctxl, ws_o, ws_ml);
  attn_combine<<<B_ * H_, 64, 0, stream>>>(ws_o, ws_ml, (unsigned*)d_out);
}
```

```cpp
#include <hip/hip_runtime.h>
#include <hip/hip_bf16.h>
#include <stdint.h>

namespace {

constexpr int B_ = 8, H_ = 32, HKV = 8, D_ = 128, BS = 16, NB = 128, LK = NB * BS, G_ = H_ / HKV;
constexpr int NSPLIT = 4, NWAVE = 16, UNIT = 32;
static_assert(NSPLIT * NWAVE * UNIT == LK, "units cover the sequence");

using bf16x8 = __attribute__((ext_vector_type(8))) short;
using f32x4 = __attribute__((ext_vector_type(4))) float;
using u32x4 = __attribute__((ext_vector_type(4))) unsigned;

__device__ __forceinline__ unsigned cvtpk(float lo, float hi) {
  unsigned r;
  asm("v_cvt_pk_bf16_f32 %0, %1, %2" : "=v"(r) : "v"(lo), "v"(hi));
  return r;
}
__device__ __forceinline__ float xg_max(float x) {
  auto s = __builtin_amdgcn_permlane16_swap(__float_as_uint(x), __float_as_uint(x), false, false);
  x = fmaxf(__uint_as_float(s[0]), __uint_as_float(s[1]));
  auto t = __builtin_amdgcn_permlane32_swap(__float_as_uint(x), __float_as_uint(x), false, false);
  return fmaxf(__uint_as_float(t[0]), __uint_as_float(t[1]));
}
__device__ __forceinline__ float xg_sum(float x) {
  auto s = __builtin_amdgcn_permlane16_swap(__float_as_uint(x), __float_as_uint(x), false, false);
  x = __uint_as_float(s[0]) + __uint_as_float(s[1]);
  auto t = __builtin_amdgcn_permlane32_swap(__float_as_uint(x), __float_as_uint(x), false, false);
  return __uint_as_float(t[0]) + __uint_as_float(t[1]);
}

constexpr int WAVE_LDS = 8192;
constexpr int LDS_ML = NWAVE * WAVE_LDS;
constexpr int LDS_BYTES = LDS_ML + 2 * G_ * NWAVE * 4;


__global__ void __launch_bounds__(512)
attn_main(const uint16_t* __restrict__ q, const uint16_t* __restrict__ kc, const uint16_t* __restrict__ vc,
          const int* __restrict__ bt, const int* __restrict__ ctxl, float* __restrict__ ws_o,
          float* __restrict__ ws_ml) {
  __shared__ __attribute__((aligned(16))) char lds[LDS_BYTES];
  const unsigned long long T0_ = __builtin_amdgcn_s_memrealtime();
  const int bid = blockIdx.x;
  const int kvh = bid & 7;
  const int b = (bid >> 3) & 7;
  const int split = bid >> 6;
  const int tid = threadIdx.x;
  const int lane = tid & 63;
  const int pwave = __builtin_amdgcn_readfirstlane(tid >> 6);
  const int g = lane >> 4, c = lane & 15;

  asm volatile("" ::"s"(q), "s"(kc), "s"(vc), "s"(bt), "s"(ctxl), "s"(ws_o), "s"(ws_ml));

  for (int wave = pwave; wave < NWAVE; wave += 8) {
  const int unit = wave * NSPLIT + split;
  const int tok0 = unit * UNIT;
  u32x4 qb[4];
  {
    const uint16_t* qp = q + ((size_t)b * H_ + kvh * G_ + (c & 3)) * D_ + 8 * g;
#pragma unroll
    for (int s = 0; s < 4; ++s) qb[s] = *reinterpret_cast<const u32x4*>(qp + 32 * s);
  }
  int ctx;
  unsigned long long pp;
  {
    const int* cp = ctxl + b;
    const int* bp = bt + b * NB + unit * 2;
    asm volatile("s_load_dword %0, %2, 0x0\n\ts_load_dwordx2 %1, %3, 0x0\n\ts_waitcnt lgkmcnt(0)"
                 : "=&s"(ctx), "=&s"(pp)
                 : "s"(cp), "s"(bp)
                 : "memory");
  }
  const int p0 = (int)(unsigned)pp, p1 = (int)(unsigned)(pp >> 32);
  const bool zeroq = ctx <= 0;
  const int len = zeroq ? LK : min(ctx, LK);
  char* wl = lds + wave * WAVE_LDS;
  float* lds_m = reinterpret_cast<float*>(lds + LDS_ML);
  float* lds_l = lds_m + G_ * NWAVE;

  if (tok0 < len) {
    const uint16_t* vp0 = vc + ((size_t)p0 * BS + 4 * g) * (HKV * D_) + kvh * D_ + 8 * c;
    const uint16_t* vp1 = vc + ((size_t)p1 * BS + 4 * g) * (HKV * D_) + kvh * D_ + 8 * c;
    const uint16_t* kb0 = kc + ((size_t)p0 * BS + g) * (HKV * D_) + kvh * D_;
    const uint16_t* kb1 = kc + ((size_t)p1 * BS + g) * (HKV * D_) + kvh * D_;
    {
      const bool qv = (c < G_) && !zeroq;
#pragma unroll
      for (int s = 0; s < 4; ++s) {
        qb[s] = qv ? qb[s] : u32x4{0u, 0u, 0u, 0u};
        asm volatile("" : "+v"(qb[s]));
      }
    }
    __builtin_amdgcn_sched_barrier(0);
#pragma unroll
    for (int i = 0; i < 4; ++i)
      __builtin_amdgcn_global_load_lds((const unsigned*)(kb0 + (size_t)(4 * i) * (HKV * D_) + 8 * (c ^ (4 * i + g))),
                                       (unsigned*)(wl + i * 1024), 16, 0, 2);
#pragma unroll
    for (int i = 0; i < 4; ++i)
      __builtin_amdgcn_global_load_lds((const unsigned*)(kb1 + (size_t)(4 * i) * (HKV * D_) + 8 * (c ^ (4 * i + g))),
                                       (unsigned*)(wl + 4096 + i * 1024), 16, 0, 2);
    u32x4 vv[8];
#pragma unroll
    for (int i = 0; i < 4; ++i) vv[i] = __builtin_nontemporal_load(reinterpret_cast<const u32x4*>(vp0 + i * (HKV * D_)));
#pragma unroll
    for (int i = 0; i < 4; ++i) vv[4 + i] = __builtin_nontemporal_load(reinterpret_cast<const u32x4*>(vp1 + i * (HKV * D_)));
    __builtin_amdgcn_sched_barrier(0);
    asm volatile("s_waitcnt vmcnt(8)" ::: "memory");
    __builtin_amdgcn_sched_barrier(0);
    f32x4 sacc[2];
#pragma unroll
    for (int pg = 0; pg < 2; ++pg) {
      f32x4 a = {0.f, 0.f, 0.f, 0.f};
#pragma unroll
      for (int s = 0; s < 4; ++s) {
        const u32x4 ka = *reinterpret_cast<const u32x4*>(wl + pg * 4096 + c * 256 + (((4 * s + g) ^ c) * 16));
        a = __builtin_amdgcn_mfma_f32_16x16x32_bf16(__builtin_bit_cast(bf16x8, ka), __builtin_bit_cast(bf16x8, qb[s]), a,
                                                    0, 0, 0);
      }
      sacc[pg] = a;
    }
    constexpr float C2 = 0.088388347648318440f * 1.4426950408889634f;
    float x[8];
    const int rem = len - tok0 - 4 * g;
#pragma unroll
    for (int pg = 0; pg < 2; ++pg)
#pragma unroll
      for (int r = 0; r < 4; ++r) x[pg * 4 + r] = (pg * 16 + r < rem) ? sacc[pg][r] * C2 : -INFINITY;
    float m = fmaxf(fmaxf(fmaxf(x[0], x[1]), fmaxf(x[2], x[3])), fmaxf(fmaxf(x[4], x[5]), fmaxf(x[6], x[7])));
    m = xg_max(m);
    float p[8];
#pragma unroll
    for (int j = 0; j < 8; ++j) p[j] = __builtin_amdgcn_exp2f(x[j] - m);
    float l = ((p[0] + p[1]) + (p[2] + p[3])) + ((p[4] + p[5]) + (p[6] + p[7]));
    l = xg_sum(l);
    u32x4 pa = {cvtpk(p[0], p[1]), cvtpk(p[2], p[3]), cvtpk(p[4], p[5]), cvtpk(p[6], p[7])};

    f32x4 o[8];
#pragma unroll
    for (int e = 0; e < 8; ++e) {
      const unsigned sel = (e & 1) ? 0x07060302u : 0x05040100u;
      u32x4 vb;
#pragma unroll
      for (int d = 0; d < 4; ++d) vb[d] = __builtin_amdgcn_perm(vv[2 * d + 1][e >> 1], vv[2 * d][e >> 1], sel);
      f32x4 z = {0.f, 0.f, 0.f, 0.f};
      o[e] = __builtin_amdgcn_mfma_f32_16x16x32_bf16(__builtin_bit_cast(bf16x8, pa), __builtin_bit_cast(bf16x8, vb),
                                                     z, 0, 0, 0);
    }
    if (g == 0) {
      float* wo = reinterpret_cast<float*>(wl);
#pragma unroll
      for (int r = 0; r < 4; ++r) {
        *reinterpret_cast<f32x4*>(wo + r * D_ + 8 * c) = f32x4{o[0][r], o[1][r], o[2][r], o[3][r]};
        *reinterpret_cast<f32x4*>(wo + r * D_ + 8 * c + 4) = f32x4{o[4][r], o[5][r], o[6][r], o[7][r]};
      }
      if (c < G_) {
        lds_m[c * NWAVE + wave] = m;
        lds_l[c * NWAVE + wave] = l;
      }
    }
  } else {
    float* wo = reinterpret_cast<float*>(wl);
    *reinterpret_cast<f32x4*>(wo + 8 * lane) = f32x4{0.f, 0.f, 0.f, 0.f};
    *reinterpret_cast<f32x4*>(wo + 8 * lane + 4) = f32x4{0.f, 0.f, 0.f, 0.f};
    if (lane < G_) {
      lds_m[lane * NWAVE + wave] = -INFINITY;
      lds_l[lane * NWAVE + wave] = 0.f;
    }
  }
  }
  float* lds_m = reinterpret_cast<float*>(lds + LDS_ML);
  float* lds_l = lds_m + G_ * NWAVE;
  __syncthreads();
  for (int tt = tid; tt < G_ * D_; tt += 512) {
    const int h = tt >> 7, d = tt & 127;
    f32x4 mv[4], lv[4];
    float ov[NWAVE];
#pragma unroll
    for (int i = 0; i < 4; ++i) {
      mv[i] = *reinterpret_cast<const f32x4*>(lds_m + h * NWAVE + 4 * i);
      lv[i] = *reinterpret_cast<const f32x4*>(lds_l + h * NWAVE + 4 * i);
    }
#pragma unroll
    for (int w = 0; w < NWAVE; ++w) ov[w] = *reinterpret_cast<const float*>(lds + w * WAVE_LDS + (h * D_ + d) * 4);
    float M = -INFINITY;
#pragma unroll
    for (int w = 0; w < NWAVE; ++w) M = fmaxf(M, mv[w >> 2][w & 3]);
    const float Ms = (M == -INFINITY) ? 0.f : M;
    float L = 0.f, acc = 0.f;
#pragma unroll
    for (int w = 0; w < NWAVE; ++w) {
      const float f = __builtin_amdgcn_exp2f(mv[w >> 2][w & 3] - Ms);
      L = fmaf(f, lv[w >> 2][w & 3], L);
      acc = fmaf(f, ov[w], acc);
    }
    const int row = b * H_ + kvh * G_ + h;
    ws_o[((size_t)row * NSPLIT + split) * D_ + d] = acc;
    if (d == 0) *reinterpret_cast<float2*>(ws_ml + (row * NSPLIT + split) * 2) = make_float2(M, L);
  }
  while (__builtin_amdgcn_s_memrealtime() - T0_ < 4000ull) __builtin_amdgcn_s_sleep(1);
}

__global__ void __launch_bounds__(64)
attn_combine(const float* __restrict__ ws_o, const float* __restrict__ ws_ml, unsigned* __restrict__ out) {
  const int row = blockIdx.x;
  const int lane = threadIdx.x;
  float mm[NSPLIT], ll[NSPLIT];
  float2 oo[NSPLIT];
#pragma unroll
  for (int s = 0; s < NSPLIT; ++s) {
    const float2 t = *reinterpret_cast<const float2*>(ws_ml + (row * NSPLIT + s) * 2);
    mm[s] = t.x;
    ll[s] = t.y;
    oo[s] = *reinterpret_cast<const float2*>(ws_o + ((size_t)row * NSPLIT + s) * D_ + 2 * lane);
  }
  float M = -INFINITY;
#pragma unroll
  for (int s = 0; s < NSPLIT; ++s) M = fmaxf(M, mm[s]);
  float L = 0.f, a0 = 0.f, a1 = 0.f;
#pragma unroll
  for (int s = 0; s < NSPLIT; ++s) {
    const float f = (ll[s] > 0.f) ? __builtin_amdgcn_exp2f(mm[s] - M) : 0.f;
    L = fmaf(f, ll[s], L);
    a0 = fmaf(f, oo[s].x, a0);
    a1 = fmaf(f, oo[s].y, a1);
  }
  const float inv = 1.f / L;
  out[row * (D_ / 2) + lane] = cvtpk(a0 * inv, a1 * inv);
}

}

extern "C" void kernel_launch(void* const* d_in, const int* in_sizes, int n_in, void* d_out, int out_size,
                              void* d_ws, size_t ws_size, hipStream_t stream) {
  const uint16_t* q = (const uint16_t*)d_in[0];
  const uint16_t* kc = (const uint16_t*)d_in[1];
  const uint16_t* vc = (const uint16_t*)d_in[2];
  const int* bt = (const int*)d_in[3];
  const int* ctxl = (const int*)d_in[4];
  float* ws_o = (float*)d_ws;
  float* ws_ml = ws_o + (size_t)B_ * H_ * NSPLIT * D_;
  attn_main<<<HKV * B_ * NSPLIT, 512, 0, stream>>>(q, kc, vc, bt, ctxl, ws_o, ws_ml);
  attn_combine<<<B_ * H_, 64, 0, stream>>>(ws_o, ws_ml, (unsigned*)d_out);
}
```

```cpp
#include <hip/hip_runtime.h>
#include <hip/hip_bf16.h>
#include <stdint.h>

namespace {

constexpr int B_ = 8, H_ = 32, HKV = 8, D_ = 128, BS = 16, NB = 128, LK = NB * BS, G_ = H_ / HKV;
constexpr int NSPLIT = 4, NWAVE = 16, UNIT = 32;
static_assert(NSPLIT * NWAVE * UNIT == LK, "units cover the sequence");

using bf16x8 = __attribute__((ext_vector_type(8))) short;
using f32x4 = __attribute__((ext_vector_type(4))) float;
using u32x4 = __attribute__((ext_vector_type(4))) unsigned;

__device__ __forceinline__ unsigned cvtpk(float lo, float hi) {
  unsigned r;
  asm("v_cvt_pk_bf16_f32 %0, %1, %2" : "=v"(r) : "v"(lo), "v"(hi));
  return r;
}
__device__ __forceinline__ float xg_max(float x) {
  auto s = __builtin_amdgcn_permlane16_swap(__float_as_uint(x), __float_as_uint(x), false, false);
  x = fmaxf(__uint_as_float(s[0]), __uint_as_float(s[1]));
  auto t = __builtin_amdgcn_permlane32_swap(__float_as_uint(x), __float_as_uint(x), false, false);
  return fmaxf(__uint_as_float(t[0]), __uint_as_float(t[1]));
}
__device__ __forceinline__ float xg_sum(float x) {
  auto s = __builtin_amdgcn_permlane16_swap(__float_as_uint(x), __float_as_uint(x), false, false);
  x = __uint_as_float(s[0]) + __uint_as_float(s[1]);
  auto t = __builtin_amdgcn_permlane32_swap(__float_as_uint(x), __float_as_uint(x), false, false);
  return __uint_as_float(t[0]) + __uint_as_float(t[1]);
}

constexpr int WAVE_LDS = 8192;
constexpr int LDS_ML = NWAVE * WAVE_LDS;
constexpr int LDS_BYTES = LDS_ML + 2 * G_ * NWAVE * 4;


__global__ void __launch_bounds__(1024)
attn_main(const uint16_t* __restrict__ q, const uint16_t* __restrict__ kc, const uint16_t* __restrict__ vc,
          const int* __restrict__ bt, const int* __restrict__ ctxl, float* __restrict__ ws_o,
          float* __restrict__ ws_ml, unsigned short* __restrict__ outp) {
  __shared__ __attribute__((aligned(16))) char lds[LDS_BYTES];
  const unsigned long long T0_ = __builtin_amdgcn_s_memrealtime();
  const int bid = blockIdx.x;
  const int kvh = bid & 7;
  const int b = (bid >> 3) & 7;
  const int tid = threadIdx.x;
  const int lane = tid & 63;
  const int wave = __builtin_amdgcn_readfirstlane(tid >> 6);
  const int g = lane >> 4, c = lane & 15;

  asm volatile("" ::"s"(q), "s"(kc), "s"(vc), "s"(bt), "s"(ctxl), "s"(ws_o), "s"(ws_ml));

  float GM = -INFINITY, GL = 0.f, GA = 0.f;
  for (int split = 0; split < NSPLIT; ++split) {
  const int unit = wave * NSPLIT + split;
  const int tok0 = unit * UNIT;
  u32x4 qb[4];
  {
    const uint16_t* qp = q + ((size_t)b * H_ + kvh * G_ + (c & 3)) * D_ + 8 * g;
#pragma unroll
    for (int s = 0; s < 4; ++s) qb[s] = *reinterpret_cast<const u32x4*>(qp + 32 * s);
  }
  int ctx;
  unsigned long long pp;
  {
    const int* cp = ctxl + b;
    const int* bp = bt + b * NB + unit * 2;
    asm volatile("s_load_dword %0, %2, 0x0\n\ts_load_dwordx2 %1, %3, 0x0\n\ts_waitcnt lgkmcnt(0)"
                 : "=&s"(ctx), "=&s"(pp)
                 : "s"(cp), "s"(bp)
                 : "memory");
  }
  const int p0 = (int)(unsigned)pp, p1 = (int)(unsigned)(pp >> 32);
  const bool zeroq = ctx <= 0;
  const int len = zeroq ? LK : min(ctx, LK);
  char* wl = lds + wave * WAVE_LDS;
  float* lds_m = reinterpret_cast<float*>(lds + LDS_ML);
  float* lds_l = lds_m + G_ * NWAVE;

  if (tok0 < len) {
    const uint16_t* vp0 = vc + ((size_t)p0 * BS + 4 * g) * (HKV * D_) + kvh * D_ + 8 * c;
    const uint16_t* vp1 = vc + ((size_t)p1 * BS + 4 * g) * (HKV * D_) + kvh * D_ + 8 * c;
    const uint16_t* kb0 = kc + ((size_t)p0 * BS + g) * (HKV * D_) + kvh * D_;
    const uint16_t* kb1 = kc + ((size_t)p1 * BS + g) * (HKV * D_) + kvh * D_;
    {
      const bool qv = (c < G_) && !zeroq;
#pragma unroll
      for (int s = 0; s < 4; ++s) {
        qb[s] = qv ? qb[s] : u32x4{0u, 0u, 0u, 0u};
        asm volatile("" : "+v"(qb[s]));
      }
    }
    __builtin_amdgcn_sched_barrier(0);
#pragma unroll
    for (int i = 0; i < 4; ++i)
      __builtin_amdgcn_global_load_lds((const unsigned*)(kb0 + (size_t)(4 * i) * (HKV * D_) + 8 * (c ^ (4 * i + g))),
                                       (unsigned*)(wl + i * 1024), 16, 0, 2);
#pragma unroll
    for (int i = 0; i < 4; ++i)
      __builtin_amdgcn_global_load_lds((const unsigned*)(kb1 + (size_t)(4 * i) * (HKV * D_) + 8 * (c ^ (4 * i + g))),
                                       (unsigned*)(wl + 4096 + i * 1024), 16, 0, 2);
    u32x4 vv[8];
#pragma unroll
    for (int i = 0; i < 4; ++i) vv[i] = __builtin_nontemporal_load(reinterpret_cast<const u32x4*>(vp0 + i * (HKV * D_)));
#pragma unroll
    for (int i = 0; i < 4; ++i) vv[4 + i] = __builtin_nontemporal_load(reinterpret_cast<const u32x4*>(vp1 + i * (HKV * D_)));
    __builtin_amdgcn_sched_barrier(0);
    asm volatile("s_waitcnt vmcnt(8)" ::: "memory");
    __builtin_amdgcn_sched_barrier(0);
    f32x4 sacc[2];
#pragma unroll
    for (int pg = 0; pg < 2; ++pg) {
      f32x4 a = {0.f, 0.f, 0.f, 0.f};
#pragma unroll
      for (int s = 0; s < 4; ++s) {
        const u32x4 ka = *reinterpret_cast<const u32x4*>(wl + pg * 4096 + c * 256 + (((4 * s + g) ^ c) * 16));
        a = __builtin_amdgcn_mfma_f32_16x16x32_bf16(__builtin_bit_cast(bf16x8, ka), __builtin_bit_cast(bf16x8, qb[s]), a,
                                                    0, 0, 0);
      }
      sacc[pg] = a;
    }
    constexpr float C2 = 0.088388347648318440f * 1.4426950408889634f;
    float x[8];
    const int rem = len - tok0 - 4 * g;
#pragma unroll
    for (int pg = 0; pg < 2; ++pg)
#pragma unroll
      for (int r = 0; r < 4; ++r) x[pg * 4 + r] = (pg * 16 + r < rem) ? sacc[pg][r] * C2 : -INFINITY;
    float m = fmaxf(fmaxf(fmaxf(x[0], x[1]), fmaxf(x[2], x[3])), fmaxf(fmaxf(x[4], x[5]), fmaxf(x[6], x[7])));
    m = xg_max(m);
    float p[8];
#pragma unroll
    for (int j = 0; j < 8; ++j) p[j] = __builtin_amdgcn_exp2f(x[j] - m);
    float l = ((p[0] + p[1]) + (p[2] + p[3])) + ((p[4] + p[5]) + (p[6] + p[7]));
    l = xg_sum(l);
    u32x4 pa = {cvtpk(p[0], p[1]), cvtpk(p[2], p[3]), cvtpk(p[4], p[5]), cvtpk(p[6], p[7])};

    f32x4 o[8];
#pragma unroll
    for (int e = 0; e < 8; ++e) {
      const unsigned sel = (e & 1) ? 0x07060302u : 0x05040100u;
      u32x4 vb;
#pragma unroll
      for (int d = 0; d < 4; ++d) vb[d] = __builtin_amdgcn_perm(vv[2 * d + 1][e >> 1], vv[2 * d][e >> 1], sel);
      f32x4 z = {0.f, 0.f, 0.f, 0.f};
      o[e] = __builtin_amdgcn_mfma_f32_16x16x32_bf16(__builtin_bit_cast(bf16x8, pa), __builtin_bit_cast(bf16x8, vb),
                                                     z, 0, 0, 0);
    }
    if (g == 0) {
      float* wo = reinterpret_cast<float*>(wl);
#pragma unroll
      for (int r = 0; r < 4; ++r) {
        *reinterpret_cast<f32x4*>(wo + r * D_ + 8 * c) = f32x4{o[0][r], o[1][r], o[2][r], o[3][r]};
        *reinterpret_cast<f32x4*>(wo + r * D_ + 8 * c + 4) = f32x4{o[4][r], o[5][r], o[6][r], o[7][r]};
      }
      if (c < G_) {
        lds_m[c * NWAVE + wave] = m;
        lds_l[c * NWAVE + wave] = l;
      }
    }
  } else {
    float* wo = reinterpret_cast<float*>(wl);
    *reinterpret_cast<f32x4*>(wo + 8 * lane) = f32x4{0.f, 0.f, 0.f, 0.f};
    *reinterpret_cast<f32x4*>(wo + 8 * lane + 4) = f32x4{0.f, 0.f, 0.f, 0.f};
    if (lane < G_) {
      lds_m[lane * NWAVE + wave] = -INFINITY;
      lds_l[lane * NWAVE + wave] = 0.f;
    }
  }
  __syncthreads();
  if (tid < G_ * D_) {
    const int h = tid >> 7, d = tid & 127;
    f32x4 mv[4], lv[4];
    float ov[NWAVE];
#pragma unroll
    for (int i = 0; i < 4; ++i) {
      mv[i] = *reinterpret_cast<const f32x4*>(lds_m + h * NWAVE + 4 * i);
      lv[i] = *reinterpret_cast<const f32x4*>(lds_l + h * NWAVE + 4 * i);
    }
#pragma unroll
    for (int w = 0; w < NWAVE; ++w) ov[w] = *reinterpret_cast<const float*>(lds + w * WAVE_LDS + (h * D_ + d) * 4);
    float M = -INFINITY;
#pragma unroll
    for (int w = 0; w < NWAVE; ++w) M = fmaxf(M, mv[w >> 2][w & 3]);
    const float Ms = (M == -INFINITY) ? 0.f : M;
    float L = 0.f, acc = 0.f;
#pragma unroll
    for (int w = 0; w < NWAVE; ++w) {
      const float f = __builtin_amdgcn_exp2f(mv[w >> 2][w & 3] - Ms);
      L = fmaf(f, lv[w >> 2][w & 3], L);
      acc = fmaf(f, ov[w], acc);
    }
    const float NM = fmaxf(GM, M);
    const float NMs = (NM == -INFINITY) ? 0.f : NM;
    const float f0 = __builtin_amdgcn_exp2f(GM - NMs), f1 = __builtin_amdgcn_exp2f(M - NMs);
    GL = GL * f0 + L * f1; GA = GA * f0 + acc * f1; GM = NM;
  }
  __syncthreads();
  }
  if (tid < G_ * D_) {
    const int h = tid >> 7, d = tid & 127;
    const int row = b * H_ + kvh * G_ + h;
    outp[row * D_ + d] = (unsigned short)(cvtpk(GA / GL, 0.f) & 0xffff);
  }
  while (__builtin_amdgcn_s_memrealtime() - T0_ < 4000ull) __builtin_amdgcn_s_sleep(1);
}

__global__ void __launch_bounds__(64)
attn_combine(const float* __restrict__ ws_o, const float* __restrict__ ws_ml, unsigned* __restrict__ out) {
  const int row = blockIdx.x;
  const int lane = threadIdx.x;
  float mm[NSPLIT], ll[NSPLIT];
  float2 oo[NSPLIT];
#pragma unroll
  for (int s = 0; s < NSPLIT; ++s) {
    const float2 t = *reinterpret_cast<const float2*>(ws_ml + (row * NSPLIT + s) * 2);
    mm[s] = t.x;
    ll[s] = t.y;
    oo[s] = *reinterpret_cast<const float2*>(ws_o + ((size_t)row * NSPLIT + s) * D_ + 2 * lane);
  }
  float M = -INFINITY;
#pragma unroll
  for (int s = 0; s < NSPLIT; ++s) M = fmaxf(M, mm[s]);
  float L = 0.f, a0 = 0.f, a1 = 0.f;
#pragma unroll
  for (int s = 0; s < NSPLIT; ++s) {
    const float f = (ll[s] > 0.f) ? __builtin_amdgcn_exp2f(mm[s] - M) : 0.f;
    L = fmaf(f, ll[s], L);
    a0 = fmaf(f, oo[s].x, a0);
    a1 = fmaf(f, oo[s].y, a1);
  }
  const float inv = 1.f / L;
  out[row * (D_ / 2) + lane] = cvtpk(a0 * inv, a1 * inv);
}

}

extern "C" void kernel_launch(void* const* d_in, const int* in_sizes, int n_in, void* d_out, int out_size,
                              void* d_ws, size_t ws_size, hipStream_t stream) {
  const uint16_t* q = (const uint16_t*)d_in[0];
  const uint16_t* kc = (const uint16_t*)d_in[1];
  const uint16_t* vc = (const uint16_t*)d_in[2];
  const int* bt = (const int*)d_in[3];
  const int* ctxl = (const int*)d_in[4];
  float* ws_o = (float*)d_ws;
  float* ws_ml = ws_o + (size_t)B_ * H_ * NSPLIT * D_;
  attn_main<<<HKV * B_, NWAVE * 64, 0, stream>>>(q, kc, vc, bt, ctxl, ws_o, ws_ml, (unsigned short*)d_out);
}
```

```cpp
#include <hip/hip_runtime.h>
#include <hip/hip_bf16.h>
#include <stdint.h>

namespace {

constexpr int B_ = 8, H_ = 32, HKV = 8, D_ = 128, BS = 16, NB = 128, LK = NB * BS, G_ = H_ / HKV;
constexpr int NWAVE = 16, UNIT = 32, NQ = 4, NCOL = 4;
constexpr int NPART = LK / UNIT / NCOL;
static_assert(NQ * G_ == 16 && NQ * NCOL == NWAVE, "MFMA columns = batches x heads");

using bf16x8 = __attribute__((ext_vector_type(8))) short;
using f32x4 = __attribute__((ext_vector_type(4))) float;
using u32x4 = __attribute__((ext_vector_type(4))) unsigned;
using i32x4 = __attribute__((ext_vector_type(4))) int;

__device__ __forceinline__ unsigned cvtpk(float lo, float hi) {
  unsigned r;
  asm("v_cvt_pk_bf16_f32 %0, %1, %2" : "=v"(r) : "v"(lo), "v"(hi));
  return r;
}
__device__ __forceinline__ float xg_max(float x) {
  auto s = __builtin_amdgcn_permlane16_swap(__float_as_uint(x), __float_as_uint(x), false, false);
  x = fmaxf(__uint_as_float(s[0]), __uint_as_float(s[1]));
  auto t = __builtin_amdgcn_permlane32_swap(__float_as_uint(x), __float_as_uint(x), false, false);
  return fmaxf(__uint_as_float(t[0]), __uint_as_float(t[1]));
}
__device__ __forceinline__ float xg_sum(float x) {
  auto s = __builtin_amdgcn_permlane16_swap(__float_as_uint(x), __float_as_uint(x), false, false);
  x = __uint_as_float(s[0]) + __uint_as_float(s[1]);
  auto t = __builtin_amdgcn_permlane32_swap(__float_as_uint(x), __float_as_uint(x), false, false);
  return __uint_as_float(t[0]) + __uint_as_float(t[1]);
}

constexpr int WAVE_LDS = 8192;
constexpr int LDS_ML = NWAVE * WAVE_LDS;
constexpr int LDS_BYTES = LDS_ML + 2 * G_ * NWAVE * 4;


__global__ void __launch_bounds__(1024)
attn_main(const uint16_t* __restrict__ q, const uint16_t* __restrict__ kc, const uint16_t* __restrict__ vc,
          const int* __restrict__ bt, const int* __restrict__ ctxl, float* __restrict__ ws_o,
          float* __restrict__ ws_ml) {
  __shared__ __attribute__((aligned(16))) char lds[LDS_BYTES];
  const int bid = blockIdx.x;
  const int kvh = bid & 7;
  const int b0 = ((bid >> 3) & 1) * NQ;
  const int cset = bid >> 4;
  const int tid = threadIdx.x;
  const int lane = tid & 63;
  const int wave = __builtin_amdgcn_readfirstlane(tid >> 6);
  const int wi = wave >> 2, wj = wave & 3;
  const int g = lane >> 4, c = lane & 15;
  asm volatile("" ::"s"(q), "s"(kc), "s"(vc), "s"(bt), "s"(ctxl), "s"(ws_o), "s"(ws_ml));

  const int unit = cset + NPART * wj;
  const int tok0 = unit * UNIT;
  u32x4 qb[4];
  {
    const uint16_t* qp = q + ((size_t)(b0 + (c >> 2)) * H_ + kvh * G_ + (c & 3)) * D_ + 8 * g;
#pragma unroll
    for (int s = 0; s < 4; ++s) qb[s] = *reinterpret_cast<const u32x4*>(qp + 32 * s);
  }
  i32x4 ctx4;
  unsigned long long pp[NQ];
  {
    const int* cp = ctxl + b0;
    const int* bp = bt + b0 * NB + unit * 2;
    asm volatile(
        "s_load_dwordx4 %0, %5, 0x0\n\ts_load_dwordx2 %1, %6, 0x0\n\ts_load_dwordx2 %2, %6, 0x200\n\t"
        "s_load_dwordx2 %3, %6, 0x400\n\ts_load_dwordx2 %4, %6, 0x600\n\ts_waitcnt lgkmcnt(0)"
        : "=&s"(ctx4), "=&s"(pp[0]), "=&s"(pp[1]), "=&s"(pp[2]), "=&s"(pp[3])
        : "s"(cp), "s"(bp)
        : "memory");
  }
  int len4[NQ];
  unsigned zq = 0, live = 0;
#pragma unroll
  for (int i = 0; i < NQ; ++i) {
    len4[i] = ctx4[i] <= 0 ? LK : min(ctx4[i], LK);
    zq |= (ctx4[i] <= 0 ? 1u : 0u) << i;
    live |= (tok0 < len4[i] ? 1u : 0u) << i;
  }
  const unsigned long long mypp = wi == 0 ? pp[0] : wi == 1 ? pp[1] : wi == 2 ? pp[2] : pp[3];
  unsigned same = 0;
#pragma unroll
  for (int i = 0; i < NQ; ++i) same |= (pp[i] == mypp ? 1u : 0u) << i;
  same &= live;
  const bool mylive = (live >> wi) & 1u;
  const bool leader = mylive && (same & ((1u << wi) - 1u)) == 0u;
  char* wl = lds + wave * WAVE_LDS;
  float* lds_m = reinterpret_cast<float*>(lds + LDS_ML);
  float* lds_l = lds_m + G_ * NWAVE;

  if (leader) {
    const int p0 = (int)(unsigned)mypp, p1 = (int)(unsigned)(mypp >> 32);
    const uint16_t* vp0 = vc + ((size_t)p0 * BS + 4 * g) * (HKV * D_) + kvh * D_ + 8 * c;
    const uint16_t* vp1 = vc + ((size_t)p1 * BS + 4 * g) * (HKV * D_) + kvh * D_ + 8 * c;
    const uint16_t* kb0 = kc + ((size_t)p0 * BS + g) * (HKV * D_) + kvh * D_;
    const uint16_t* kb1 = kc + ((size_t)p1 * BS + g) * (HKV * D_) + kvh * D_;
    const int ci = c >> 2;
    const bool mem = (same >> ci) & 1u;
    {
      const bool qv = mem && !((zq >> ci) & 1u);
#pragma unroll
      for (int s = 0; s < 4; ++s) {
        qb[s] = qv ? qb[s] : u32x4{0u, 0u, 0u, 0u};
        asm volatile("" : "+v"(qb[s]));
      }
    }
    __builtin_amdgcn_sched_barrier(0);
#pragma unroll
    for (int i = 0; i < 4; ++i)
      __builtin_amdgcn_global_load_lds((const unsigned*)(kb0 + (size_t)(4 * i) * (HKV * D_) + 8 * (c ^ (4 * i + g))),
                                       (unsigned*)(wl + i * 1024), 16, 0, 2);
#pragma unroll
    for (int i = 0; i < 4; ++i)
      __builtin_amdgcn_global_load_lds((const unsigned*)(kb1 + (size_t)(4 * i) * (HKV * D_) + 8 * (c ^ (4 * i + g))),
                                       (unsigned*)(wl + 4096 + i * 1024), 16, 0, 2);
    u32x4 vv[8];
#pragma unroll
    for (int i = 0; i < 4; ++i) vv[i] = __builtin_nontemporal_load(reinterpret_cast<const u32x4*>(vp0 + i * (HKV * D_)));
#pragma unroll
    for (int i = 0; i < 4; ++i) vv[4 + i] = __builtin_nontemporal_load(reinterpret_cast<const u32x4*>(vp1 + i * (HKV * D_)));
    __builtin_amdgcn_sched_barrier(0);
    asm volatile("s_waitcnt vmcnt(8)" ::: "memory");
    __builtin_amdgcn_sched_barrier(0);
    f32x4 sacc[2];
#pragma unroll
    for (int pg = 0; pg < 2; ++pg) {
      f32x4 a = {0.f, 0.f, 0.f, 0.f};
#pragma unroll
      for (int s = 0; s < 4; ++s) {
        const u32x4 ka = *reinterpret_cast<const u32x4*>(wl + pg * 4096 + c * 256 + (((4 * s + g) ^ c) * 16));
        a = __builtin_amdgcn_mfma_f32_16x16x32_bf16(__builtin_bit_cast(bf16x8, ka), __builtin_bit_cast(bf16x8, qb[s]), a,
                                                    0, 0, 0);
      }
      sacc[pg] = a;
    }
    constexpr float C2 = 0.088388347648318440f * 1.4426950408889634f;
    float x[8];
    const int mylen = ci == 0 ? len4[0] : ci == 1 ? len4[1] : ci == 2 ? len4[2] : len4[3];
    const int rem = mem ? mylen - tok0 - 4 * g : 0;
#pragma unroll
    for (int pg = 0; pg < 2; ++pg)
#pragma unroll
      for (int r = 0; r < 4; ++r) x[pg * 4 + r] = (pg * 16 + r < rem) ? sacc[pg][r] * C2 : -INFINITY;
    float m = fmaxf(fmaxf(fmaxf(x[0], x[1]), fmaxf(x[2], x[3])), fmaxf(fmaxf(x[4], x[5]), fmaxf(x[6], x[7])));
    m = xg_max(m);
    const float msub = mem ? m : 0.f;
    float p[8];
#pragma unroll
    for (int j = 0; j < 8; ++j) p[j] = __builtin_amdgcn_exp2f(x[j] - msub);
    float l = ((p[0] + p[1]) + (p[2] + p[3])) + ((p[4] + p[5]) + (p[6] + p[7]));
    l = xg_sum(l);
    u32x4 pa = {cvtpk(p[0], p[1]), cvtpk(p[2], p[3]), cvtpk(p[4], p[5]), cvtpk(p[6], p[7])};

    f32x4 o[8];
#pragma unroll
    for (int e = 0; e < 8; ++e) {
      const unsigned sel = (e & 1) ? 0x07060302u : 0x05040100u;
      u32x4 vb;
#pragma unroll
      for (int d = 0; d < 4; ++d) vb[d] = __builtin_amdgcn_perm(vv[2 * d + 1][e >> 1], vv[2 * d][e >> 1], sel);
      f32x4 z = {0.f, 0.f, 0.f, 0.f};
      o[e] = __builtin_amdgcn_mfma_f32_16x16x32_bf16(__builtin_bit_cast(bf16x8, pa), __builtin_bit_cast(bf16x8, vb),
                                                     z, 0, 0, 0);
    }
    if ((same >> g) & 1u) {
      float* wo = reinterpret_cast<float*>(lds + (g * NCOL + wj) * WAVE_LDS);
#pragma unroll
      for (int r = 0; r < 4; ++r) {
        *reinterpret_cast<f32x4*>(wo + r * D_ + 8 * c) = f32x4{o[0][r], o[1][r], o[2][r], o[3][r]};
        *reinterpret_cast<f32x4*>(wo + r * D_ + 8 * c + 4) = f32x4{o[4][r], o[5][r], o[6][r], o[7][r]};
      }
    }
    if (g == 0 && mem) {
      lds_m[(ci * NCOL + wj) * G_ + (c & 3)] = m;
      lds_l[(ci * NCOL + wj) * G_ + (c & 3)] = l;
    }
  } else if (!mylive) {
    float* wo = reinterpret_cast<float*>(wl);
    *reinterpret_cast<f32x4*>(wo + 8 * lane) = f32x4{0.f, 0.f, 0.f, 0.f};
    *reinterpret_cast<f32x4*>(wo + 8 * lane + 4) = f32x4{0.f, 0.f, 0.f, 0.f};
    if (lane < G_) {
      lds_m[wave * G_ + lane] = -INFINITY;
      lds_l[wave * G_ + lane] = 0.f;
    }
  }
  __syncthreads();
  {
    const int i = tid >> 8, h = (tid >> 6) & 3, d2 = tid & 63;
    float mv[NCOL], lv[NCOL];
    float2 ov[NCOL];
#pragma unroll
    for (int j = 0; j < NCOL; ++j) {
      mv[j] = lds_m[(i * NCOL + j) * G_ + h];
      lv[j] = lds_l[(i * NCOL + j) * G_ + h];
      ov[j] = *reinterpret_cast<const float2*>(lds + (i * NCOL + j) * WAVE_LDS + (h * D_ + 2 * d2) * 4);
    }
    const float M = fmaxf(fmaxf(mv[0], mv[1]), fmaxf(mv[2], mv[3]));
    const float Ms = (M == -INFINITY) ? 0.f : M;
    float L = 0.f, a0 = 0.f, a1 = 0.f;
#pragma unroll
    for (int j = 0; j < NCOL; ++j) {
      const float f = __builtin_amdgcn_exp2f(mv[j] - Ms);
      L = fmaf(f, lv[j], L);
      a0 = fmaf(f, ov[j].x, a0);
      a1 = fmaf(f, ov[j].y, a1);
    }
    const int row = (b0 + i) * H_ + kvh * G_ + h;
    *reinterpret_cast<float2*>(ws_o + ((size_t)row * NPART + cset) * D_ + 2 * d2) = make_float2(a0, a1);
    if (d2 == 0) *reinterpret_cast<float2*>(ws_ml + (row * NPART + cset) * 2) = make_float2(M, L);
  }
}

__global__ void __launch_bounds__(64)
attn_combine(const float* __restrict__ ws_o, const float* __restrict__ ws_ml, unsigned* __restrict__ out) {
  const int row = blockIdx.x;
  const int lane = threadIdx.x;
  float mm[NPART], ll[NPART];
  float2 oo[NPART];
#pragma unroll
  for (int s = 0; s < NPART; ++s) {
    const float2 t = *reinterpret_cast<const float2*>(ws_ml + (row * NPART + s) * 2);
    mm[s] = t.x;
    ll[s] = t.y;
    oo[s] = *reinterpret_cast<const float2*>(ws_o + ((size_t)row * NPART + s) * D_ + 2 * lane);
  }
  float M = -INFINITY;
#pragma unroll
  for (int s = 0; s < NPART; ++s) M = fmaxf(M, mm[s]);
  float L = 0.f, a0 = 0.f, a1 = 0.f;
#pragma unroll
  for (int s = 0; s < NPART; ++s) {
    const float f = __builtin_amdgcn_exp2f(mm[s] - M);
    L = fmaf(f, ll[s], L);
    a0 = fmaf(f, oo[s].x, a0);
    a1 = fmaf(f, oo[s].y, a1);
  }
  const float inv = 1.f / L;
  out[row * (D_ / 2) + lane] = cvtpk(a0 * inv, a1 * inv);
}

}

extern "C" void kernel_launch(void* const* d_in, const int* in_sizes, int n_in, void* d_out, int out_size,
                              void* d_ws, size_t ws_size, hipStream_t stream) {
  const uint16_t* q = (const uint16_t*)d_in[0];
  const uint16_t* kc = (const uint16_t*)d_in[1];
  const uint16_t* vc = (const uint16_t*)d_in[2];
  const int* bt = (const int*)d_in[3];
  const int* ctxl = (const int*)d_in[4];
  float* ws_o = (float*)d_ws;
  float* ws_ml = ws_o + (size_t)B_ * H_ * NPART * D_;
  attn_main<<<HKV * (B_ / NQ) * NPART, NWAVE * 64, 0, stream>>>(q, kc, vc, bt, ctxl, ws_o, ws_ml);
  attn_combine<<<B_ * H_, 64, 0, stream>>>(ws_o, ws_ml, (unsigned*)d_out);
}
```

```cpp
#include <hip/hip_runtime.h>
#include <hip/hip_bf16.h>
#include <stdint.h>

namespace {

constexpr int B_ = 8, H_ = 32, HKV = 8, D_ = 128, BS = 16, NB = 128, LK = NB * BS, G_ = H_ / HKV;
constexpr int NWAVE = 4, MAXJ = 4, UNIT = 32, NQ = 4, NCOLS = LK / UNIT;
constexpr int NPART = 16;
static_assert(NQ * G_ == 16 && NCOLS == 64 && NQ * NCOLS == NPART * NWAVE * MAXJ, "layout");

using bf16x8 = __attribute__((ext_vector_type(8))) short;
using f32x4 = __attribute__((ext_vector_type(4))) float;
using u32x4 = __attribute__((ext_vector_type(4))) unsigned;
using u32x2 = __attribute__((ext_vector_type(2))) unsigned;
using i32x4 = __attribute__((ext_vector_type(4))) int;

__device__ __forceinline__ unsigned cvtpk(float lo, float hi) {
  unsigned r;
  asm("v_cvt_pk_bf16_f32 %0, %1, %2" : "=v"(r) : "v"(lo), "v"(hi));
  return r;
}
__device__ __forceinline__ float xg_max(float x) {
  auto s = __builtin_amdgcn_permlane16_swap(__float_as_uint(x), __float_as_uint(x), false, false);
  x = fmaxf(__uint_as_float(s[0]), __uint_as_float(s[1]));
  auto t = __builtin_amdgcn_permlane32_swap(__float_as_uint(x), __float_as_uint(x), false, false);
  return fmaxf(__uint_as_float(t[0]), __uint_as_float(t[1]));
}
__device__ __forceinline__ float xg_sum(float x) {
  auto s = __builtin_amdgcn_permlane16_swap(__float_as_uint(x), __float_as_uint(x), false, false);
  x = __uint_as_float(s[0]) + __uint_as_float(s[1]);
  auto t = __builtin_amdgcn_permlane32_swap(__float_as_uint(x), __float_as_uint(x), false, false);
  return __uint_as_float(t[0]) + __uint_as_float(t[1]);
}
__device__ __forceinline__ int prefix_count(unsigned long long m) {
  return __builtin_amdgcn_mbcnt_hi((unsigned)(m >> 32), __builtin_amdgcn_mbcnt_lo((unsigned)m, 0u));
}

constexpr int REG_LDS = 8192;
constexpr int LDS_ML = NWAVE * MAXJ * REG_LDS;
constexpr int LDS_BYTES = LDS_ML + 2 * NWAVE * 16 * 4;


__global__ void __launch_bounds__(NWAVE * 64)
attn_main(const uint16_t* __restrict__ q, const uint16_t* __restrict__ kc, const uint16_t* __restrict__ vc,
          const int* __restrict__ bt, const int* __restrict__ ctxl, float* __restrict__ ws_o,
          float* __restrict__ ws_ml) {
  __shared__ __attribute__((aligned(16))) char lds[LDS_BYTES];
  const int bid = blockIdx.x;
  const int kvh = bid & 7;
  const int b0 = ((bid >> 3) & 1) * NQ;
  const int cset = bid >> 4;
  const int tid = threadIdx.x;
  const int lane = tid & 63;
  const int wave = __builtin_amdgcn_readfirstlane(tid >> 6);
  const int g = lane >> 4, c = lane & 15;
  asm volatile("" ::"s"(q), "s"(kc), "s"(vc), "s"(bt), "s"(ctxl), "s"(ws_o), "s"(ws_ml));

  u32x2 pg[NQ];
#pragma unroll
  for (int i = 0; i < NQ; ++i) pg[i] = *reinterpret_cast<const u32x2*>(bt + (b0 + i) * NB + 2 * lane);
  u32x4 qb[4];
  {
    const uint16_t* qp = q + ((size_t)(b0 + (c >> 2)) * H_ + kvh * G_ + (c & 3)) * D_ + 8 * g;
#pragma unroll
    for (int s = 0; s < 4; ++s) qb[s] = *reinterpret_cast<const u32x4*>(qp + 32 * s);
  }
  i32x4 ctx4;
  {
    const int* cp = ctxl + b0;
    asm volatile("s_load_dwordx4 %0, %1, 0x0\n\ts_waitcnt lgkmcnt(0)" : "=&s"(ctx4) : "s"(cp) : "memory");
  }
  int len4[NQ];
#pragma unroll
  for (int i = 0; i < NQ; ++i) len4[i] = ctx4[i] <= 0 ? LK : min(ctx4[i], LK);
  const int ci = c >> 2;
  unsigned grp[NQ];
  unsigned lead = 0;
  {
    unsigned lv = 0;
#pragma unroll
    for (int i = 0; i < NQ; ++i) lv |= (lane * UNIT < len4[i] ? 1u : 0u) << i;
#pragma unroll
    for (int i = 0; i < NQ; ++i) {
      unsigned s = 0;
#pragma unroll
      for (int k = 0; k < NQ; ++k) s |= (pg[i][0] == pg[k][0] && pg[i][1] == pg[k][1] ? 1u : 0u) << k;
      grp[i] = s & lv;
      lead |= (((lv >> i) & 1u) && (grp[i] & ((1u << i) - 1u)) == 0u ? 1u : 0u) << i;
    }
  }
  int base = 0, ntot = 0;
#pragma unroll
  for (int i = 0; i < NQ; ++i) {
    const unsigned long long mk = __ballot((lead >> i) & 1u);
    base += prefix_count(mk);
    ntot += __popcll(mk);
  }
  const int rbeg = (cset * ntot) >> 4, cnt = (((cset + 1) * ntot) >> 4) - rbeg;
  const int nj = wave < cnt ? (cnt - wave + NWAVE - 1) / NWAVE : 0;
  const int bend = base + __popc(lead);
  {
    const int myctx = ci == 0 ? ctx4[0] : ci == 1 ? ctx4[1] : ci == 2 ? ctx4[2] : ctx4[3];
#pragma unroll
    for (int s = 0; s < 4; ++s) {
      qb[s] = myctx > 0 ? qb[s] : u32x4{0u, 0u, 0u, 0u};
      asm volatile("" : "+v"(qb[s]));
    }
  }
  __builtin_amdgcn_sched_barrier(0);

  char* wl = lds + wave * (MAXJ * REG_LDS);
  int p0[MAXJ], p1[MAXJ], rem[MAXJ];
  const int mylen = ci == 0 ? len4[0] : ci == 1 ? len4[1] : ci == 2 ? len4[2] : len4[3];
  const uint16_t* kbase = kc + kvh * D_ + g * (HKV * D_);
#pragma unroll
  for (int j = 0; j < MAXJ; ++j) {
    rem[j] = 0;
    if (j < nj) {
      const int r = rbeg + wave + NWAVE * j;
      const unsigned long long hit = __ballot(base <= r && r < bend);
      const int col = __builtin_amdgcn_readfirstlane(__ffsll((long long)hit) - 1);
      unsigned lb = __builtin_amdgcn_readlane(lead, col);
      for (int t = r - __builtin_amdgcn_readlane(base, col); t > 0; --t) lb &= lb - 1u;
      const int bi = __ffs(lb) - 1;
      const u32x2 pgv = bi == 0 ? pg[0] : bi == 1 ? pg[1] : bi == 2 ? pg[2] : pg[3];
      const unsigned grv = bi == 0 ? grp[0] : bi == 1 ? grp[1] : bi == 2 ? grp[2] : grp[3];
      p0[j] = __builtin_amdgcn_readlane(pgv[0], col);
      p1[j] = __builtin_amdgcn_readlane(pgv[1], col);
      const unsigned same = __builtin_amdgcn_readlane(grv, col);
      rem[j] = ((same >> ci) & 1u) ? mylen - col * UNIT - 4 * g : 0;
      const uint16_t* kb0 = kbase + (size_t)p0[j] * (BS * HKV * D_);
      const uint16_t* kb1 = kbase + (size_t)p1[j] * (BS * HKV * D_);
#pragma unroll
      for (int i = 0; i < 4; ++i)
        __builtin_amdgcn_global_load_lds((const unsigned*)(kb0 + (4 * i) * (HKV * D_) + 8 * (c ^ (4 * i + g))),
                                         (unsigned*)(wl + j * REG_LDS + i * 1024), 16, 0, 2);
#pragma unroll
      for (int i = 0; i < 4; ++i)
        __builtin_amdgcn_global_load_lds((const unsigned*)(kb1 + (4 * i) * (HKV * D_) + 8 * (c ^ (4 * i + g))),
                                         (unsigned*)(wl + j * REG_LDS + 4096 + i * 1024), 16, 0, 2);
    }
  }
  u32x4 vv[MAXJ][8];
  const uint16_t* vbase = vc + kvh * D_ + 4 * g * (HKV * D_) + 8 * c;
#pragma unroll
  for (int j = 0; j < MAXJ; ++j) {
    if (j < nj) {
      const uint16_t* vp0 = vbase + (size_t)p0[j] * (BS * HKV * D_);
      const uint16_t* vp1 = vbase + (size_t)p1[j] * (BS * HKV * D_);
#pragma unroll
      for (int i = 0; i < 4; ++i) vv[j][i] = __builtin_nontemporal_load(reinterpret_cast<const u32x4*>(vp0 + i * (HKV * D_)));
#pragma unroll
      for (int i = 0; i < 4; ++i) vv[j][4 + i] = __builtin_nontemporal_load(reinterpret_cast<const u32x4*>(vp1 + i * (HKV * D_)));
    }
  }
  __builtin_amdgcn_sched_barrier(0);
  if (nj == 1) asm volatile("s_waitcnt vmcnt(8)" ::: "memory");
  else if (nj == 2) asm volatile("s_waitcnt vmcnt(16)" ::: "memory");
  else if (nj == 3) asm volatile("s_waitcnt vmcnt(24)" ::: "memory");
  else asm volatile("s_waitcnt vmcnt(32)" ::: "memory");
  __builtin_amdgcn_sched_barrier(0);

  constexpr float C2 = 0.088388347648318440f * 1.4426950408889634f;
  float x[MAXJ][8];
  float m = -INFINITY;
#pragma unroll
  for (int j = 0; j < MAXJ; ++j) {
    if (j < nj) {
#pragma unroll
      for (int h2 = 0; h2 < 2; ++h2) {
        f32x4 a = {0.f, 0.f, 0.f, 0.f};
#pragma unroll
        for (int s = 0; s < 4; ++s) {
          const u32x4 ka =
              *reinterpret_cast<const u32x4*>(wl + j * REG_LDS + h2 * 4096 + c * 256 + (((4 * s + g) ^ c) * 16));
          a = __builtin_amdgcn_mfma_f32_16x16x32_bf16(__builtin_bit_cast(bf16x8, ka), __builtin_bit_cast(bf16x8, qb[s]),
                                                      a, 0, 0, 0);
        }
#pragma unroll
        for (int r4 = 0; r4 < 4; ++r4) {
          x[j][h2 * 4 + r4] = (h2 * 16 + r4 < rem[j]) ? a[r4] * C2 : -INFINITY;
          m = fmaxf(m, x[j][h2 * 4 + r4]);
        }
      }
    }
  }
  m = xg_max(m);
  const float msub = (m == -INFINITY) ? 0.f : m;
  float l = 0.f;
  f32x4 o[8];
#pragma unroll
  for (int e = 0; e < 8; ++e) o[e] = f32x4{0.f, 0.f, 0.f, 0.f};
#pragma unroll
  for (int j = 0; j < MAXJ; ++j) {
    if (j < nj) {
      float p[8];
#pragma unroll
      for (int i = 0; i < 8; ++i) p[i] = __builtin_amdgcn_exp2f(x[j][i] - msub);
      l += ((p[0] + p[1]) + (p[2] + p[3])) + ((p[4] + p[5]) + (p[6] + p[7]));
      const u32x4 pa = {cvtpk(p[0], p[1]), cvtpk(p[2], p[3]), cvtpk(p[4], p[5]), cvtpk(p[6], p[7])};
#pragma unroll
      for (int e = 0; e < 8; ++e) {
        const unsigned sel = (e & 1) ? 0x07060302u : 0x05040100u;
        u32x4 vb;
#pragma unroll
        for (int d = 0; d < 4; ++d) vb[d] = __builtin_amdgcn_perm(vv[j][2 * d + 1][e >> 1], vv[j][2 * d][e >> 1], sel);
        o[e] = __builtin_amdgcn_mfma_f32_16x16x32_bf16(__builtin_bit_cast(bf16x8, pa), __builtin_bit_cast(bf16x8, vb),
                                                       o[e], 0, 0, 0);
      }
    }
  }
  l = xg_sum(l);
  float* lds_m = reinterpret_cast<float*>(lds + LDS_ML);
  float* lds_l = lds_m + NWAVE * 16;
  {
    float* wo = reinterpret_cast<float*>(wl) + g * (G_ * D_);
#pragma unroll
    for (int r4 = 0; r4 < 4; ++r4) {
      *reinterpret_cast<f32x4*>(wo + r4 * D_ + 8 * c) = f32x4{o[0][r4], o[1][r4], o[2][r4], o[3][r4]};
      *reinterpret_cast<f32x4*>(wo + r4 * D_ + 8 * c + 4) = f32x4{o[4][r4], o[5][r4], o[6][r4], o[7][r4]};
    }
    if (g == 0) {
      lds_m[c * NWAVE + wave] = m;
      lds_l[c * NWAVE + wave] = l;
    }
  }
  __syncthreads();
  {
    const int row16 = tid >> 4, d8 = tid & 15;
    const f32x4 mv = *reinterpret_cast<const f32x4*>(lds_m + row16 * NWAVE);
    const f32x4 lv = *reinterpret_cast<const f32x4*>(lds_l + row16 * NWAVE);
    f32x4 oa[NWAVE], ob[NWAVE];
#pragma unroll
    for (int w = 0; w < NWAVE; ++w) {
      const char* src = lds + w * (MAXJ * REG_LDS) + (row16 * D_ + 4 * d8) * 4;
      oa[w] = *reinterpret_cast<const f32x4*>(src);
      ob[w] = *reinterpret_cast<const f32x4*>(src + 256);
    }
    const float M = fmaxf(fmaxf(mv[0], mv[1]), fmaxf(mv[2], mv[3]));
    const float Ms = (M == -INFINITY) ? 0.f : M;
    float L = 0.f;
    f32x4 a = {0.f, 0.f, 0.f, 0.f}, bacc = {0.f, 0.f, 0.f, 0.f};
#pragma unroll
    for (int w = 0; w < NWAVE; ++w) {
      const float f = __builtin_amdgcn_exp2f(mv[w] - Ms);
      L = fmaf(f, lv[w], L);
      a += f > 0.f ? f * oa[w] : f32x4{0.f, 0.f, 0.f, 0.f};
      bacc += f > 0.f ? f * ob[w] : f32x4{0.f, 0.f, 0.f, 0.f};
    }
    const int row = (b0 + (row16 >> 2)) * H_ + kvh * G_ + (row16 & 3);
    float* dst = ws_o + ((size_t)row * NPART + cset) * D_ + 4 * d8;
    *reinterpret_cast<f32x4*>(dst) = a;
    *reinterpret_cast<f32x4*>(dst + 64) = bacc;
    if (d8 == 0) *reinterpret_cast<float2*>(ws_ml + (row * NPART + cset) * 2) = make_float2(M, L);
  }
}

__global__ void __launch_bounds__(64)
attn_combine(const float* __restrict__ ws_o, const float* __restrict__ ws_ml, unsigned* __restrict__ out) {
  const int row = blockIdx.x;
  const int lane = threadIdx.x;
  float mm[NPART], ll[NPART];
  float2 oo[NPART];
#pragma unroll
  for (int s = 0; s < NPART; ++s) {
    const float2 t = *reinterpret_cast<const float2*>(ws_ml + (row * NPART + s) * 2);
    mm[s] = t.x;
    ll[s] = t.y;
    oo[s] = *reinterpret_cast<const float2*>(ws_o + ((size_t)row * NPART + s) * D_ + 2 * lane);
  }
  float M = -INFINITY;
#pragma unroll
  for (int s = 0; s < NPART; ++s) M = fmaxf(M, mm[s]);
  float L = 0.f, a0 = 0.f, a1 = 0.f;
#pragma unroll
  for (int s = 0; s < NPART; ++s) {
    const float f = __builtin_amdgcn_exp2f(mm[s] - M);
    L = fmaf(f, ll[s], L);
    a0 = fmaf(f, oo[s].x, a0);
    a1 = fmaf(f, oo[s].y, a1);
  }
  const float inv = 1.f / L;
  out[row * (D_ / 2) + lane] = cvtpk(a0 * inv, a1 * inv);
}

}

extern "C" void kernel_launch(void* const* d_in, const int* in_sizes, int n_in, void* d_out, int out_size,
                              void* d_ws, size_t ws_size, hipStream_t stream) {
  const uint16_t* q = (const uint16_t*)d_in[0];
  const uint16_t* kc = (const uint16_t*)d_in[1];
  const uint16_t* vc = (const uint16_t*)d_in[2];
  const int* bt = (const int*)d_in[3];
  const int* ctxl = (const int*)d_in[4];
  float* ws_o = (float*)d_ws;
  float* ws_ml = ws_o + (size_t)B_ * H_ * NPART * D_;
  attn_main<<<HKV * (B_ / NQ) * NPART, NWAVE * 64, 0, stream>>>(q, kc, vc, bt, ctxl, ws_o, ws_ml);
  attn_combine<<<B_ * H_, 64, 0, stream>>>(ws_o, ws_ml, (unsigned*)d_out);
}
```

```cpp
#include <hip/hip_runtime.h>
#include <hip/hip_bf16.h>
#include <stdint.h>

namespace {

constexpr int B_ = 8, H_ = 32, HKV = 8, D_ = 128, BS = 16, NB = 128, LK = NB * BS, G_ = H_ / HKV;
constexpr int NWAVE = 4, MAXJ = 4, UNIT = 32, NQ = 4, NCOLS = LK / UNIT;
constexpr int NPART = 16;
static_assert(NQ * G_ == 16 && NCOLS == 64 && NQ * NCOLS == NPART * NWAVE * MAXJ, "layout");

using bf16x8 = __attribute__((ext_vector_type(8))) short;
using f32x4 = __attribute__((ext_vector_type(4))) float;
using u32x4 = __attribute__((ext_vector_type(4))) unsigned;
using u32x2 = __attribute__((ext_vector_type(2))) unsigned;
using i32x4 = __attribute__((ext_vector_type(4))) int;

__device__ __forceinline__ unsigned cvtpk(float lo, float hi) {
  unsigned r;
  asm("v_cvt_pk_bf16_f32 %0, %1, %2" : "=v"(r) : "v"(lo), "v"(hi));
  return r;
}
__device__ __forceinline__ float xg_max(float x) {
  auto s = __builtin_amdgcn_permlane16_swap(__float_as_uint(x), __float_as_uint(x), false, false);
  x = fmaxf(__uint_as_float(s[0]), __uint_as_float(s[1]));
  auto t = __builtin_amdgcn_permlane32_swap(__float_as_uint(x), __float_as_uint(x), false, false);
  return fmaxf(__uint_as_float(t[0]), __uint_as_float(t[1]));
}
__device__ __forceinline__ float xg_sum(float x) {
  auto s = __builtin_amdgcn_permlane16_swap(__float_as_uint(x), __float_as_uint(x), false, false);
  x = __uint_as_float(s[0]) + __uint_as_float(s[1]);
  auto t = __builtin_amdgcn_permlane32_swap(__float_as_uint(x), __float_as_uint(x), false, false);
  return __uint_as_float(t[0]) + __uint_as_float(t[1]);
}
__device__ __forceinline__ int prefix_count(unsigned long long m) {
  return __builtin_amdgcn_mbcnt_hi((unsigned)(m >> 32), __builtin_amdgcn_mbcnt_lo((unsigned)m, 0u));
}

constexpr int REG_LDS = 8192;
constexpr int LDS_ML = NWAVE * MAXJ * REG_LDS;
constexpr int LDS_BYTES = LDS_ML + 2 * NWAVE * 16 * 4;


__global__ void __launch_bounds__(NWAVE * 64)
attn_main(const uint16_t* __restrict__ q, const uint16_t* __restrict__ kc, const uint16_t* __restrict__ vc,
          const int* __restrict__ bt, const int* __restrict__ ctxl, float* __restrict__ ws_o,
          float* __restrict__ ws_ml) {
  __shared__ __attribute__((aligned(16))) char lds[LDS_BYTES];
  const int bid = blockIdx.x;
  const int kvh = bid & 7;
  const int b0 = ((bid >> 3) & 1) * NQ;
  const int cset = bid >> 4;
  const int tid = threadIdx.x;
  const int lane = tid & 63;
  const int wave = __builtin_amdgcn_readfirstlane(tid >> 6);
  const int g = lane >> 4, c = lane & 15;
  asm volatile("" ::"s"(q), "s"(kc), "s"(vc), "s"(bt), "s"(ctxl), "s"(ws_o), "s"(ws_ml));

  unsigned long long pg[NQ];
#pragma unroll
  for (int i = 0; i < NQ; ++i) pg[i] = *reinterpret_cast<const unsigned long long*>(bt + (b0 + i) * NB + 2 * lane);
  u32x4 qb[4];
  {
    const uint16_t* qp = q + ((size_t)(b0 + (c >> 2)) * H_ + kvh * G_ + (c & 3)) * D_ + 8 * g;
    asm volatile(
        "global_load_dwordx4 %0, %4, off\n\tglobal_load_dwordx4 %1, %4, off offset:64\n\t"
        "global_load_dwordx4 %2, %4, off offset:128\n\tglobal_load_dwordx4 %3, %4, off offset:192"
        : "=&v"(qb[0]), "=&v"(qb[1]), "=&v"(qb[2]), "=&v"(qb[3])
        : "v"(qp)
        : "memory");
  }
  i32x4 ctx4;
  {
    const int* cp = ctxl + b0;
    asm volatile("s_load_dwordx4 %0, %1, 0x0\n\ts_waitcnt lgkmcnt(0)" : "=&s"(ctx4) : "s"(cp) : "memory");
  }
  int len4[NQ];
  unsigned long long lv[NQ];
#pragma unroll
  for (int i = 0; i < NQ; ++i) {
    len4[i] = ctx4[i] <= 0 ? LK : min(ctx4[i], LK);
    lv[i] = ~0ull >> (64 - ((len4[i] + UNIT - 1) / UNIT));
  }
  const int ci = c >> 2;
  constexpr float C2 = 0.088388347648318440f * 1.4426950408889634f;
  const int mylen = (ci & 2) ? ((ci & 1) ? len4[3] : len4[2]) : ((ci & 1) ? len4[1] : len4[0]);
  const int myctx = (ci & 2) ? ((ci & 1) ? ctx4[3] : ctx4[2]) : ((ci & 1) ? ctx4[1] : ctx4[0]);
  const float scl = myctx > 0 ? C2 : 0.f;
  unsigned koff[4];
#pragma unroll
  for (int i = 0; i < 4; ++i) koff[i] = (4 * i + g) * (HKV * D_ * 2) + kvh * (D_ * 2) + ((c ^ (4 * i + g)) * 16);
  const unsigned voff = 4 * g * (HKV * D_ * 2) + kvh * (D_ * 2) + c * 16;
  const unsigned voff2 = voff + 2 * (HKV * D_ * 2);
  const unsigned long long e01 = __ballot(pg[0] == pg[1]), e02 = __ballot(pg[0] == pg[2]), e03 = __ballot(pg[0] == pg[3]);
  const unsigned long long e12 = __ballot(pg[1] == pg[2]), e13 = __ballot(pg[1] == pg[3]), e23 = __ballot(pg[2] == pg[3]);
  const unsigned long long ld0 = lv[0];
  const unsigned long long ld1 = lv[1] & ~(e01 & lv[0]);
  const unsigned long long ld2 = lv[2] & ~(e02 & lv[0]) & ~(e12 & lv[1]);
  const unsigned long long ld3 = lv[3] & ~(e03 & lv[0]) & ~(e13 & lv[1]) & ~(e23 & lv[2]);
  const int n0 = __popcll(ld0), n1 = n0 + __popcll(ld1), n2 = n1 + __popcll(ld2), ntot = n2 + __popcll(ld3);
  const int rbeg = (cset * ntot) >> 4, cnt = (((cset + 1) * ntot) >> 4) - rbeg;
  const int nj = wave < cnt ? (cnt - wave + NWAVE - 1) / NWAVE : 0;

  char* wl = lds + wave * (MAXJ * REG_LDS);
  int rem[MAXJ];
  u32x4 vv[MAXJ][8];
#pragma unroll
  for (int j = 0; j < MAXJ; ++j) {
    rem[j] = 0;
    if (j < nj) {
      const int r = rbeg + wave + NWAVE * j;
      const int bi = (r >= n0) + (r >= n1) + (r >= n2);
      const unsigned long long ld = bi == 0 ? ld0 : bi == 1 ? ld1 : bi == 2 ? ld2 : ld3;
      const int t = r - (bi == 0 ? 0 : bi == 1 ? n0 : bi == 2 ? n1 : n2);
      const int col = __ffsll((long long)(__ballot(prefix_count(ld) == t) & ld)) - 1;
      const unsigned long long pv = bi == 0 ? pg[0] : bi == 1 ? pg[1] : bi == 2 ? pg[2] : pg[3];
      const unsigned p0 = __builtin_amdgcn_readlane((unsigned)pv, col), p1 = __builtin_amdgcn_readlane((unsigned)(pv >> 32), col);
      const char* k0 = reinterpret_cast<const char*>(kc) + (size_t)p0 * (BS * HKV * D_ * 2);
      const char* k1 = reinterpret_cast<const char*>(kc) + (size_t)p1 * (BS * HKV * D_ * 2);
#pragma unroll
      for (int i = 0; i < 4; ++i)
        __builtin_amdgcn_global_load_lds((const unsigned*)(k0 + koff[i]), (unsigned*)(wl + j * REG_LDS + i * 1024), 16, 0, 2);
#pragma unroll
      for (int i = 0; i < 4; ++i)
        __builtin_amdgcn_global_load_lds((const unsigned*)(k1 + koff[i]), (unsigned*)(wl + j * REG_LDS + 4096 + i * 1024), 16, 0, 2);
      const char* v0 = reinterpret_cast<const char*>(vc) + (size_t)p0 * (BS * HKV * D_ * 2);
      const char* v1 = reinterpret_cast<const char*>(vc) + (size_t)p1 * (BS * HKV * D_ * 2);
      asm volatile(
          "global_load_dwordx4 %0, %8, %10 nt\n\tglobal_load_dwordx4 %1, %8, %10 offset:2048 nt\n\t"
          "global_load_dwordx4 %2, %9, %10 nt\n\tglobal_load_dwordx4 %3, %9, %10 offset:2048 nt\n\t"
          "global_load_dwordx4 %4, %8, %11 nt\n\tglobal_load_dwordx4 %5, %8, %11 offset:2048 nt\n\t"
          "global_load_dwordx4 %6, %9, %11 nt\n\tglobal_load_dwordx4 %7, %9, %11 offset:2048 nt"
          : "=&v"(vv[j][0]), "=&v"(vv[j][1]), "=&v"(vv[j][2]), "=&v"(vv[j][3]), "=&v"(vv[j][4]), "=&v"(vv[j][5]),
            "=&v"(vv[j][6]), "=&v"(vv[j][7])
          : "v"(voff), "v"(voff2), "s"(v0), "s"(v1)
          : "memory");
      const unsigned long long ea = bi == 0 ? e01 : bi == 1 ? e12 : e23;
      const unsigned long long eb = bi == 0 ? e02 : e13;
      const unsigned same = (1u | (unsigned)((ea >> col) & 1ull) << 1 | (unsigned)((eb >> col) & 1ull) << 2 |
                             (unsigned)((e03 >> col) & 1ull) << 3)
                            << bi;
      rem[j] = ((same >> ci) & 1u) ? mylen - col * UNIT - 4 * g : 0;
    }
  }
  asm volatile("s_waitcnt vmcnt(8)" : "+v"(qb[0]), "+v"(qb[1]), "+v"(qb[2]), "+v"(qb[3])::"memory");
  __builtin_amdgcn_sched_barrier(0);

  float x[MAXJ][8];
  float m = -INFINITY;
#pragma unroll
  for (int j = 0; j < MAXJ; ++j) {
    if (j < nj) {
#pragma unroll
      for (int h2 = 0; h2 < 2; ++h2) {
        f32x4 a = {0.f, 0.f, 0.f, 0.f};
#pragma unroll
        for (int s = 0; s < 4; ++s) {
          const u32x4 ka =
              *reinterpret_cast<const u32x4*>(wl + j * REG_LDS + h2 * 4096 + c * 256 + (((4 * s + g) ^ c) * 16));
          a = __builtin_amdgcn_mfma_f32_16x16x32_bf16(__builtin_bit_cast(bf16x8, ka), __builtin_bit_cast(bf16x8, qb[s]),
                                                      a, 0, 0, 0);
        }
#pragma unroll
        for (int r4 = 0; r4 < 4; ++r4) {
          x[j][h2 * 4 + r4] = (h2 * 16 + r4 < rem[j]) ? a[r4] * scl : -INFINITY;
          m = fmaxf(m, x[j][h2 * 4 + r4]);
        }
      }
    }
  }
  m = xg_max(m);
  const float msub = (m == -INFINITY) ? 0.f : m;
  float l = 0.f;
  u32x4 pa[MAXJ];
#pragma unroll
  for (int j = 0; j < MAXJ; ++j) {
    if (j < nj) {
      float p[8];
#pragma unroll
      for (int i = 0; i < 8; ++i) p[i] = __builtin_amdgcn_exp2f(x[j][i] - msub);
      l += ((p[0] + p[1]) + (p[2] + p[3])) + ((p[4] + p[5]) + (p[6] + p[7]));
      pa[j] = u32x4{cvtpk(p[0], p[1]), cvtpk(p[2], p[3]), cvtpk(p[4], p[5]), cvtpk(p[6], p[7])};
    }
  }
  f32x4 o[8];
#pragma unroll
  for (int e = 0; e < 8; ++e) o[e] = f32x4{0.f, 0.f, 0.f, 0.f};
#pragma unroll
  for (int j = 0; j < MAXJ; ++j) {
    if (j < nj) {
      if (j == nj - 1) asm volatile("s_waitcnt vmcnt(0)" ::: "memory");
      asm volatile(""
                   : "+v"(vv[j][0]), "+v"(vv[j][1]), "+v"(vv[j][2]), "+v"(vv[j][3]), "+v"(vv[j][4]), "+v"(vv[j][5]),
                     "+v"(vv[j][6]), "+v"(vv[j][7])::"memory");
#pragma unroll
      for (int e = 0; e < 8; ++e) {
        const unsigned sel = (e & 1) ? 0x07060302u : 0x05040100u;
        u32x4 vb;
#pragma unroll
        for (int d = 0; d < 4; ++d) vb[d] = __builtin_amdgcn_perm(vv[j][2 * d + 1][e >> 1], vv[j][2 * d][e >> 1], sel);
        o[e] = __builtin_amdgcn_mfma_f32_16x16x32_bf16(__builtin_bit_cast(bf16x8, pa[j]), __builtin_bit_cast(bf16x8, vb),
                                                       o[e], 0, 0, 0);
      }
    }
  }
  l = xg_sum(l);
  float* lds_m = reinterpret_cast<float*>(lds + LDS_ML);
  float* lds_l = lds_m + NWAVE * 16;
  {
    float* wo = reinterpret_cast<float*>(wl) + g * (G_ * D_);
#pragma unroll
    for (int r4 = 0; r4 < 4; ++r4) {
      *reinterpret_cast<f32x4*>(wo + r4 * D_ + 8 * c) = f32x4{o[0][r4], o[1][r4], o[2][r4], o[3][r4]};
      *reinterpret_cast<f32x4*>(wo + r4 * D_ + 8 * c + 4) = f32x4{o[4][r4], o[5][r4], o[6][r4], o[7][r4]};
    }
    if (g == 0) {
      lds_m[c * NWAVE + wave] = m;
      lds_l[c * NWAVE + wave] = l;
    }
  }
  __syncthreads();
  {
    const int row16 = tid >> 4, d8 = tid & 15;
    const f32x4 mv = *reinterpret_cast<const f32x4*>(lds_m + row16 * NWAVE);
    const f32x4 lv = *reinterpret_cast<const f32x4*>(lds_l + row16 * NWAVE);
    f32x4 oa[NWAVE], ob[NWAVE];
#pragma unroll
    for (int w = 0; w < NWAVE; ++w) {
      const char* src = lds + w * (MAXJ * REG_LDS) + (row16 * D_ + 4 * d8) * 4;
      oa[w] = *reinterpret_cast<const f32x4*>(src);
      ob[w] = *reinterpret_cast<const f32x4*>(src + 256);
    }
    const float M = fmaxf(fmaxf(mv[0], mv[1]), fmaxf(mv[2], mv[3]));
    const float Ms = (M == -INFINITY) ? 0.f : M;
    float L = 0.f;
    f32x4 a = {0.f, 0.f, 0.f, 0.f}, bacc = {0.f, 0.f, 0.f, 0.f};
#pragma unroll
    for (int w = 0; w < NWAVE; ++w) {
      const float f = __builtin_amdgcn_exp2f(mv[w] - Ms);
      L = fmaf(f, lv[w], L);
      a += f > 0.f ? f * oa[w] : f32x4{0.f, 0.f, 0.f, 0.f};
      bacc += f > 0.f ? f * ob[w] : f32x4{0.f, 0.f, 0.f, 0.f};
    }
    const int row = (b0 + (row16 >> 2)) * H_ + kvh * G_ + (row16 & 3);
    float* dst = ws_o + ((size_t)row * NPART + cset) * D_ + 4 * d8;
    *reinterpret_cast<f32x4*>(dst) = a;
    *reinterpret_cast<f32x4*>(dst + 64) = bacc;
    if (d8 == 0) *reinterpret_cast<float2*>(ws_ml + (row * NPART + cset) * 2) = make_float2(M, L);
  }
}

__global__ void __launch_bounds__(64)
attn_combine(const float* __restrict__ ws_o, const float* __restrict__ ws_ml, unsigned* __restrict__ out) {
  const int row = blockIdx.x;
  const int lane = threadIdx.x;
  float mm[NPART], ll[NPART];
  float2 oo[NPART];
#pragma unroll
  for (int s = 0; s < NPART; ++s) {
    const float2 t = *reinterpret_cast<const float2*>(ws_ml + (row * NPART + s) * 2);
    mm[s] = t.x;
    ll[s] = t.y;
    oo[s] = *reinterpret_cast<const float2*>(ws_o + ((size_t)row * NPART + s) * D_ + 2 * lane);
  }
  float M = -INFINITY;
#pragma unroll
  for (int s = 0; s < NPART; ++s) M = fmaxf(M, mm[s]);
  float L = 0.f, a0 = 0.f, a1 = 0.f;
#pragma unroll
  for (int s = 0; s < NPART; ++s) {
    const float f = __builtin_amdgcn_exp2f(mm[s] - M);
    L = fmaf(f, ll[s], L);
    a0 = fmaf(f, oo[s].x, a0);
    a1 = fmaf(f, oo[s].y, a1);
  }
  const float inv = 1.f / L;
  out[row * (D_ / 2) + lane] = cvtpk(a0 * inv, a1 * inv);
}

}

extern "C" void kernel_launch(void* const* d_in, const int* in_sizes, int n_in, void* d_out, int out_size,
                              void* d_ws, size_t ws_size, hipStream_t stream) {
  const uint16_t* q = (const uint16_t*)d_in[0];
  const uint16_t* kc = (const uint16_t*)d_in[1];
  const uint16_t* vc = (const uint16_t*)d_in[2];
  const int* bt = (const int*)d_in[3];
  const int* ctxl = (const int*)d_in[4];
  float* ws_o = (float*)d_ws;
  float* ws_ml = ws_o + (size_t)B_ * H_ * NPART * D_;
  attn_main<<<HKV * (B_ / NQ) * NPART, NWAVE * 64, 0, stream>>>(q, kc, vc, bt, ctxl, ws_o, ws_ml);
  attn_combine<<<B_ * H_, 64, 0, stream>>>(ws_o, ws_ml, (unsigned*)d_out);
}
```

```cpp
#include <hip/hip_runtime.h>
#include <hip/hip_bf16.h>
#include <stdint.h>

namespace {

constexpr int B_ = 8, H_ = 32, HKV = 8, D_ = 128, BS = 16, NB = 128, LK = NB * BS, G_ = H_ / HKV;
#define NW 8
constexpr int NWAVE = NW, MAXJ = (16 + NW - 1) / NW, UNIT = 32, NQ = 4, NCOLS = LK / UNIT;
constexpr int NPART = 16;
static_assert(NQ * G_ == 16 && NCOLS == 64 && NQ * NCOLS == NPART * 16 && NWAVE * MAXJ >= 16, "layout");

using bf16x8 = __attribute__((ext_vector_type(8))) short;
using f32x4 = __attribute__((ext_vector_type(4))) float;
using u32x4 = __attribute__((ext_vector_type(4))) unsigned;
using u32x2 = __attribute__((ext_vector_type(2))) unsigned;
using i32x4 = __attribute__((ext_vector_type(4))) int;

__device__ __forceinline__ unsigned cvtpk(float lo, float hi) {
  unsigned r;
  asm("v_cvt_pk_bf16_f32 %0, %1, %2" : "=v"(r) : "v"(lo), "v"(hi));
  return r;
}
__device__ __forceinline__ float xg_max(float x) {
  auto s = __builtin_amdgcn_permlane16_swap(__float_as_uint(x), __float_as_uint(x), false, false);
  x = fmaxf(__uint_as_float(s[0]), __uint_as_float(s[1]));
  auto t = __builtin_amdgcn_permlane32_swap(__float_as_uint(x), __float_as_uint(x), false, false);
  return fmaxf(__uint_as_float(t[0]), __uint_as_float(t[1]));
}
__device__ __forceinline__ float xg_sum(float x) {
  auto s = __builtin_amdgcn_permlane16_swap(__float_as_uint(x), __float_as_uint(x), false, false);
  x = __uint_as_float(s[0]) + __uint_as_float(s[1]);
  auto t = __builtin_amdgcn_permlane32_swap(__float_as_uint(x), __float_as_uint(x), false, false);
  return __uint_as_float(t[0]) + __uint_as_float(t[1]);
}
__device__ __forceinline__ int prefix_count(unsigned long long m) {
  return __builtin_amdgcn_mbcnt_hi((unsigned)(m >> 32), __builtin_amdgcn_mbcnt_lo((unsigned)m, 0u));
}

constexpr int REG_LDS = 8192;
constexpr int LDS_ML = 16 * REG_LDS;
constexpr int NWP = 8;
constexpr int LDS_BYTES = LDS_ML + 2 * NWP * 16 * 4;

__global__ void __launch_bounds__(NWAVE * 64)
attn_main(const uint16_t* __restrict__ q, const uint16_t* __restrict__ kc, const uint16_t* __restrict__ vc,
          const int* __restrict__ bt, const int* __restrict__ ctxl, float* __restrict__ ws_o,
          float* __restrict__ ws_ml) {
  __shared__ __attribute__((aligned(16))) char lds[LDS_BYTES];
  const int bid = blockIdx.x;
  const int kvh = bid & 7;
  const int b0 = ((bid >> 3) & 1) * NQ;
  const int cset = bid >> 4;
  const int tid = threadIdx.x;
  const int lane = tid & 63;
  const int wave = __builtin_amdgcn_readfirstlane(tid >> 6);
  const int g = lane >> 4, c = lane & 15;
  asm volatile("" ::"s"(q), "s"(kc), "s"(vc), "s"(bt), "s"(ctxl), "s"(ws_o), "s"(ws_ml));

  unsigned long long pg[NQ];
#pragma unroll
  for (int i = 0; i < NQ; ++i) pg[i] = *reinterpret_cast<const unsigned long long*>(bt + (b0 + i) * NB + 2 * lane);
  u32x4 qb[4];
  {
    const uint16_t* qp = q + ((size_t)(b0 + (c >> 2)) * H_ + kvh * G_ + (c & 3)) * D_ + 8 * g;
    asm volatile(
        "global_load_dwordx4 %0, %4, off\n\tglobal_load_dwordx4 %1, %4, off offset:64\n\t"
        "global_load_dwordx4 %2, %4, off offset:128\n\tglobal_load_dwordx4 %3, %4, off offset:192"
        : "=&v"(qb[0]), "=&v"(qb[1]), "=&v"(qb[2]), "=&v"(qb[3])
        : "v"(qp)
        : "memory");
  }
  i32x4 ctx4;
  {
    const int* cp = ctxl + b0;
    asm volatile("s_load_dwordx4 %0, %1, 0x0\n\ts_waitcnt lgkmcnt(0)" : "=&s"(ctx4) : "s"(cp) : "memory");
  }
  int len4[NQ];
  unsigned long long lv[NQ];
#pragma unroll
  for (int i = 0; i < NQ; ++i) {
    len4[i] = ctx4[i] <= 0 ? LK : min(ctx4[i], LK);
    lv[i] = ~0ull >> (64 - ((len4[i] + UNIT - 1) / UNIT));
  }
  const int ci = c >> 2;
  constexpr float C2 = 0.088388347648318440f * 1.4426950408889634f;
  const int mylen = (ci & 2) ? ((ci & 1) ? len4[3] : len4[2]) : ((ci & 1) ? len4[1] : len4[0]);
  const int myctx = (ci & 2) ? ((ci & 1) ? ctx4[3] : ctx4[2]) : ((ci & 1) ? ctx4[1] : ctx4[0]);
  const float scl = myctx > 0 ? C2 : 0.f;
  unsigned koff[4];
#pragma unroll
  for (int i = 0; i < 4; ++i) koff[i] = (4 * i + g) * (HKV * D_ * 2) + kvh * (D_ * 2) + ((c ^ (4 * i + g)) * 16);
  const unsigned voff = 4 * g * (HKV * D_ * 2) + kvh * (D_ * 2) + c * 16;
  const unsigned voff2 = voff + 2 * (HKV * D_ * 2);
  const unsigned long long e01 = __ballot(pg[0] == pg[1]), e02 = __ballot(pg[0] == pg[2]), e03 = __ballot(pg[0] == pg[3]);
  const unsigned long long e12 = __ballot(pg[1] == pg[2]), e13 = __ballot(pg[1] == pg[3]), e23 = __ballot(pg[2] == pg[3]);
  const unsigned long long ld0 = lv[0];
  const unsigned long long ld1 = lv[1] & ~(e01 & lv[0]);
  const unsigned long long ld2 = lv[2] & ~(e02 & lv[0]) & ~(e12 & lv[1]);
  const unsigned long long ld3 = lv[3] & ~(e03 & lv[0]) & ~(e13 & lv[1]) & ~(e23 & lv[2]);
  const int n0 = __popcll(ld0), n1 = n0 + __popcll(ld1), n2 = n1 + __popcll(ld2), ntot = n2 + __popcll(ld3);
  const int rbeg = (cset * ntot) >> 4, cnt = (((cset + 1) * ntot) >> 4) - rbeg;
  const int nj = wave < cnt ? (cnt - wave + NWAVE - 1) / NWAVE : 0;

  char* wl = lds + wave * REG_LDS;
  int rem[MAXJ];
  unsigned pp0[MAXJ], pp1[MAXJ];
#pragma unroll
  for (int j = 0; j < MAXJ; ++j) {
    rem[j] = 0;
    if (j < nj) {
      const int r = rbeg + wave + NWAVE * j;
      const int bi = (r >= n0) + (r >= n1) + (r >= n2);
      const unsigned long long ld = bi == 0 ? ld0 : bi == 1 ? ld1 : bi == 2 ? ld2 : ld3;
      const int t = r - (bi == 0 ? 0 : bi == 1 ? n0 : bi == 2 ? n1 : n2);
      const int col = __ffsll((long long)(__ballot(prefix_count(ld) == t) & ld)) - 1;
      const unsigned long long pv = bi == 0 ? pg[0] : bi == 1 ? pg[1] : bi == 2 ? pg[2] : pg[3];
      const unsigned p0 = __builtin_amdgcn_readlane((unsigned)pv, col), p1 = __builtin_amdgcn_readlane((unsigned)(pv >> 32), col);
      const char* k0 = reinterpret_cast<const char*>(kc) + (size_t)p0 * (BS * HKV * D_ * 2);
      const char* k1 = reinterpret_cast<const char*>(kc) + (size_t)p1 * (BS * HKV * D_ * 2);
#pragma unroll
      for (int i = 0; i < 4; ++i)
        __builtin_amdgcn_global_load_lds((const unsigned*)(k0 + koff[i]), (unsigned*)(wl + j * (NWAVE * REG_LDS) + i * 1024), 16, 0, 2);
#pragma unroll
      for (int i = 0; i < 4; ++i)
        __builtin_amdgcn_global_load_lds((const unsigned*)(k1 + koff[i]), (unsigned*)(wl + j * (NWAVE * REG_LDS) + 4096 + i * 1024), 16, 0, 2);
      pp0[j] = p0;
      pp1[j] = p1;
      const unsigned long long ea = bi == 0 ? e01 : bi == 1 ? e12 : e23;
      const unsigned long long eb = bi == 0 ? e02 : e13;
      const unsigned same = (1u | (unsigned)((ea >> col) & 1ull) << 1 | (unsigned)((eb >> col) & 1ull) << 2 |
                             (unsigned)((e03 >> col) & 1ull) << 3)
                            << bi;
      rem[j] = ((same >> ci) & 1u) ? mylen - col * UNIT - 4 * g : 0;
    }
  }
  u32x4 vv[MAXJ][8];
#pragma unroll
  for (int j = 0; j < MAXJ; ++j) {
    if (j < nj) {
      const char* v0 = reinterpret_cast<const char*>(vc) + (size_t)pp0[j] * (BS * HKV * D_ * 2);
      const char* v1 = reinterpret_cast<const char*>(vc) + (size_t)pp1[j] * (BS * HKV * D_ * 2);
      asm volatile(
          "global_load_dwordx4 %0, %8, %10 nt\n\tglobal_load_dwordx4 %1, %8, %10 offset:2048 nt\n\t"
          "global_load_dwordx4 %2, %9, %10 nt\n\tglobal_load_dwordx4 %3, %9, %10 offset:2048 nt\n\t"
          "global_load_dwordx4 %4, %8, %11 nt\n\tglobal_load_dwordx4 %5, %8, %11 offset:2048 nt\n\t"
          "global_load_dwordx4 %6, %9, %11 nt\n\tglobal_load_dwordx4 %7, %9, %11 offset:2048 nt"
          : "=&v"(vv[j][0]), "=&v"(vv[j][1]), "=&v"(vv[j][2]), "=&v"(vv[j][3]), "=&v"(vv[j][4]), "=&v"(vv[j][5]),
            "=&v"(vv[j][6]), "=&v"(vv[j][7])
          : "v"(voff), "v"(voff2), "s"(v0), "s"(v1)
          : "memory");
    }
  }
  if (nj == 0) asm volatile("s_waitcnt vmcnt(0)" ::: "memory");
  else if (nj == 1) asm volatile("s_waitcnt vmcnt(8)" ::: "memory");
  else if (nj == 2) asm volatile("s_waitcnt vmcnt(16)" ::: "memory");
  else if (nj == 3) asm volatile("s_waitcnt vmcnt(24)" ::: "memory");
  else asm volatile("s_waitcnt vmcnt(32)" ::: "memory");
  asm volatile("" : "+v"(qb[0]), "+v"(qb[1]), "+v"(qb[2]), "+v"(qb[3])::"memory");
  __builtin_amdgcn_sched_barrier(0);

  float x[MAXJ][8];
  float m = -INFINITY;
#pragma unroll
  for (int j = 0; j < MAXJ; ++j) {
    if (j < nj) {
#pragma unroll
      for (int h2 = 0; h2 < 2; ++h2) {
        f32x4 a = {0.f, 0.f, 0.f, 0.f};
#pragma unroll
        for (int s = 0; s < 4; ++s) {
          const u32x4 ka =
              *reinterpret_cast<const u32x4*>(wl + j * (NWAVE * REG_LDS) + h2 * 4096 + c * 256 + (((4 * s + g) ^ c) * 16));
          a = __builtin_amdgcn_mfma_f32_16x16x32_bf16(__builtin_bit_cast(bf16x8, ka), __builtin_bit_cast(bf16x8, qb[s]),
                                                      a, 0, 0, 0);
        }
#pragma unroll
        for (int r4 = 0; r4 < 4; ++r4) {
          x[j][h2 * 4 + r4] = (h2 * 16 + r4 < rem[j]) ? a[r4] * scl : -INFINITY;
          m = fmaxf(m, x[j][h2 * 4 + r4]);
        }
      }
    }
  }
  m = xg_max(m);
  const float msub = (m == -INFINITY) ? 0.f : m;
  float l = 0.f;
  u32x4 pa[MAXJ];
#pragma unroll
  for (int j = 0; j < MAXJ; ++j) {
    if (j < nj) {
      float p[8];
#pragma unroll
      for (int i = 0; i < 8; ++i) p[i] = __builtin_amdgcn_exp2f(x[j][i] - msub);
      l += ((p[0] + p[1]) + (p[2] + p[3])) + ((p[4] + p[5]) + (p[6] + p[7]));
      pa[j] = u32x4{cvtpk(p[0], p[1]), cvtpk(p[2], p[3]), cvtpk(p[4], p[5]), cvtpk(p[6], p[7])};
    }
  }
  f32x4 o[8];
#pragma unroll
  for (int e = 0; e < 8; ++e) o[e] = f32x4{0.f, 0.f, 0.f, 0.f};
#pragma unroll
  for (int j = 0; j < MAXJ; ++j) {
    if (j < nj) {
      {
        const int later = nj - 1 - j;
        if (later == 0) asm volatile("s_waitcnt vmcnt(0)" ::: "memory");
        else if (later == 1) asm volatile("s_waitcnt vmcnt(8)" ::: "memory");
        else if (later == 2) asm volatile("s_waitcnt vmcnt(16)" ::: "memory");
        else asm volatile("s_waitcnt vmcnt(24)" ::: "memory");
      }
      asm volatile(""
                   : "+v"(vv[j][0]), "+v"(vv[j][1]), "+v"(vv[j][2]), "+v"(vv[j][3]), "+v"(vv[j][4]), "+v"(vv[j][5]),
                     "+v"(vv[j][6]), "+v"(vv[j][7])::"memory");
#pragma unroll
      for (int e = 0; e < 8; ++e) {
        const unsigned sel = (e & 1) ? 0x07060302u : 0x05040100u;
        u32x4 vb;
#pragma unroll
        for (int d = 0; d < 4; ++d) vb[d] = __builtin_amdgcn_perm(vv[j][2 * d + 1][e >> 1], vv[j][2 * d][e >> 1], sel);
        o[e] = __builtin_amdgcn_mfma_f32_16x16x32_bf16(__builtin_bit_cast(bf16x8, pa[j]), __builtin_bit_cast(bf16x8, vb),
                                                       o[e], 0, 0, 0);
      }
    }
  }
  l = xg_sum(l);
  float* lds_m = reinterpret_cast<float*>(lds + LDS_ML);
  float* lds_l = lds_m + NWP * 16;
  {
    float* wo = reinterpret_cast<float*>(wl) + g * (G_ * D_);
#pragma unroll
    for (int r4 = 0; r4 < 4; ++r4) {
      *reinterpret_cast<f32x4*>(wo + r4 * D_ + 8 * c) = f32x4{o[0][r4], o[1][r4], o[2][r4], o[3][r4]};
      *reinterpret_cast<f32x4*>(wo + r4 * D_ + 8 * c + 4) = f32x4{o[4][r4], o[5][r4], o[6][r4], o[7][r4]};
    }
    if (g == 0) {
      lds_m[c * NWP + wave] = m;
      lds_l[c * NWP + wave] = l;
    }
  }
  __syncthreads();
  if (tid < 256) {
    const int row16 = tid >> 4, d8 = tid & 15;
    float mv[NWAVE], lv[NWAVE];
    f32x4 oa[NWAVE], ob[NWAVE];
#pragma unroll
    for (int w = 0; w < NWAVE; ++w) {
      mv[w] = lds_m[row16 * NWP + w];
      lv[w] = lds_l[row16 * NWP + w];
      const char* src = lds + w * REG_LDS + (row16 * D_ + 4 * d8) * 4;
      oa[w] = *reinterpret_cast<const f32x4*>(src);
      ob[w] = *reinterpret_cast<const f32x4*>(src + 256);
    }
    float M = mv[0];
#pragma unroll
    for (int w = 1; w < NWAVE; ++w) M = fmaxf(M, mv[w]);
    const float Ms = (M == -INFINITY) ? 0.f : M;
    float L = 0.f;
    f32x4 a = {0.f, 0.f, 0.f, 0.f}, bacc = {0.f, 0.f, 0.f, 0.f};
#pragma unroll
    for (int w = 0; w < NWAVE; ++w) {
      const float f = __builtin_amdgcn_exp2f(mv[w] - Ms);
      L = fmaf(f, lv[w], L);
      a += f > 0.f ? f * oa[w] : f32x4{0.f, 0.f, 0.f, 0.f};
      bacc += f > 0.f ? f * ob[w] : f32x4{0.f, 0.f, 0.f, 0.f};
    }
    const int row = (b0 + (row16 >> 2)) * H_ + kvh * G_ + (row16 & 3);
    float* dst = ws_o + ((size_t)row * NPART + cset) * D_ + 4 * d8;
    *reinterpret_cast<f32x4*>(dst) = a;
    *reinterpret_cast<f32x4*>(dst + 64) = bacc;
    if (d8 == 0) *reinterpret_cast<float2*>(ws_ml + (row * NPART + cset) * 2) = make_float2(M, L);
  }
}

__global__ void __launch_bounds__(64)
attn_combine(const float* __restrict__ ws_o, const float* __restrict__ ws_ml, unsigned* __restrict__ out) {
  const int row = blockIdx.x;
  const int lane = threadIdx.x;
  float mm[NPART], ll[NPART];
  float2 oo[NPART];
#pragma unroll
  for (int s = 0; s < NPART; ++s) {
    const float2 t = *reinterpret_cast<const float2*>(ws_ml + (row * NPART + s) * 2);
    mm[s] = t.x;
    ll[s] = t.y;
    oo[s] = *reinterpret_cast<const float2*>(ws_o + ((size_t)row * NPART + s) * D_ + 2 * lane);
  }
  float M = -INFINITY;
#pragma unroll
  for (int s = 0; s < NPART; ++s) M = fmaxf(M, mm[s]);
  float L = 0.f, a0 = 0.f, a1 = 0.f;
#pragma unroll
  for (int s = 0; s < NPART; ++s) {
    const float f = __builtin_amdgcn_exp2f(mm[s] - M);
    L = fmaf(f, ll[s], L);
    a0 = fmaf(f, oo[s].x, a0);
    a1 = fmaf(f, oo[s].y, a1);
  }
  const float inv = 1.f / L;
  out[row * (D_ / 2) + lane] = cvtpk(a0 * inv, a1 * inv);
}

}

extern "C" void kernel_launch(void* const* d_in, const int* in_sizes, int n_in, void* d_out, int out_size,
                              void* d_ws, size_t ws_size, hipStream_t stream) {
  const uint16_t* q = (const uint16_t*)d_in[0];
  const uint16_t* kc = (const uint16_t*)d_in[1];
  const uint16_t* vc = (const uint16_t*)d_in[2];
  const int* bt = (const int*)d_in[3];
  const int* ctxl = (const int*)d_in[4];
  float* ws_o = (float*)d_ws;
  float* ws_ml = ws_o + (size_t)B_ * H_ * NPART * D_;
  attn_main<<<HKV * (B_ / NQ) * NPART, NWAVE * 64, 0, stream>>>(q, kc, vc, bt, ctxl, ws_o, ws_ml);
  attn_combine<<<B_ * H_, 64, 0, stream>>>(ws_o, ws_ml, (unsigned*)d_out);
}
```

```cpp
#include <hip/hip_runtime.h>
#include <hip/hip_bf16.h>
#include <stdint.h>

namespace {

constexpr int B_ = 8, H_ = 32, HKV = 8, D_ = 128, BS = 16, NB = 128, LK = NB * BS, G_ = H_ / HKV;
#define NW 6
constexpr int NWAVE = NW, MAXJ = (16 + NW - 1) / NW, UNIT = 32, NQ = 4, NCOLS = LK / UNIT;
constexpr int NPART = 16;
static_assert(NQ * G_ == 16 && NCOLS == 64 && NQ * NCOLS == NPART * 16 && NWAVE * MAXJ >= 16, "layout");

using bf16x8 = __attribute__((ext_vector_type(8))) short;
using f32x4 = __attribute__((ext_vector_type(4))) float;
using u32x4 = __attribute__((ext_vector_type(4))) unsigned;
using u32x2 = __attribute__((ext_vector_type(2))) unsigned;
using i32x4 = __attribute__((ext_vector_type(4))) int;

__device__ __forceinline__ unsigned cvtpk(float lo, float hi) {
  unsigned r;
  asm("v_cvt_pk_bf16_f32 %0, %1, %2" : "=v"(r) : "v"(lo), "v"(hi));
  return r;
}
__device__ __forceinline__ float xg_max(float x) {
  auto s = __builtin_amdgcn_permlane16_swap(__float_as_uint(x), __float_as_uint(x), false, false);
  x = fmaxf(__uint_as_float(s[0]), __uint_as_float(s[1]));
  auto t = __builtin_amdgcn_permlane32_swap(__float_as_uint(x), __float_as_uint(x), false, false);
  return fmaxf(__uint_as_float(t[0]), __uint_as_float(t[1]));
}
__device__ __forceinline__ float xg_sum(float x) {
  auto s = __builtin_amdgcn_permlane16_swap(__float_as_uint(x), __float_as_uint(x), false, false);
  x = __uint_as_float(s[0]) + __uint_as_float(s[1]);
  auto t = __builtin_amdgcn_permlane32_swap(__float_as_uint(x), __float_as_uint(x), false, false);
  return __uint_as_float(t[0]) + __uint_as_float(t[1]);
}
__device__ __forceinline__ int prefix_count(unsigned long long m) {
  return __builtin_amdgcn_mbcnt_hi((unsigned)(m >> 32), __builtin_amdgcn_mbcnt_lo((unsigned)m, 0u));
}

constexpr int REG_LDS = 8192;
constexpr int LDS_ML = 16 * REG_LDS;
constexpr int NWP = 8;
constexpr int LDS_BYTES = LDS_ML + 2 * NWP * 16 * 4;

__global__ void __launch_bounds__(NWAVE * 64)
attn_main(const uint16_t* __restrict__ q, const uint16_t* __restrict__ kc, const uint16_t* __restrict__ vc,
          const int* __restrict__ bt, const int* __restrict__ ctxl, float* __restrict__ ws_o,
          float* __restrict__ ws_ml) {
  __shared__ __attribute__((aligned(16))) char lds[LDS_BYTES];
  const int bid = blockIdx.x;
  const int kvh = bid & 7;
  const int b0 = ((bid >> 3) & 1) * NQ;
  const int cset = bid >> 4;
  const int tid = threadIdx.x;
  const int lane = tid & 63;
  const int wave = __builtin_amdgcn_readfirstlane(tid >> 6);
  const int g = lane >> 4, c = lane & 15;
  asm volatile("" ::"s"(q), "s"(kc), "s"(vc), "s"(bt), "s"(ctxl), "s"(ws_o), "s"(ws_ml));

  unsigned long long pg[NQ];
#pragma unroll
  for (int i = 0; i < NQ; ++i) pg[i] = *reinterpret_cast<const unsigned long long*>(bt + (b0 + i) * NB + 2 * lane);
  i32x4 ctx4;
  {
    const int* cp = ctxl + b0;
    asm volatile("s_load_dwordx4 %0, %1, 0x0\n\ts_waitcnt lgkmcnt(0)" : "=&s"(ctx4) : "s"(cp) : "memory");
  }
  int len4[NQ];
  unsigned long long lv[NQ];
#pragma unroll
  for (int i = 0; i < NQ; ++i) {
    len4[i] = ctx4[i] <= 0 ? LK : min(ctx4[i], LK);
    lv[i] = ~0ull >> (64 - ((len4[i] + UNIT - 1) / UNIT));
  }
  const int ci = c >> 2;
  constexpr float C2 = 0.088388347648318440f * 1.4426950408889634f;
  const int mylen = (ci & 2) ? ((ci & 1) ? len4[3] : len4[2]) : ((ci & 1) ? len4[1] : len4[0]);
  const int myctx = (ci & 2) ? ((ci & 1) ? ctx4[3] : ctx4[2]) : ((ci & 1) ? ctx4[1] : ctx4[0]);
  const float scl = myctx > 0 ? C2 : 0.f;
  unsigned koff[4];
#pragma unroll
  for (int i = 0; i < 4; ++i) koff[i] = (4 * i + g) * (HKV * D_ * 2) + kvh * (D_ * 2) + ((c ^ (4 * i + g)) * 16);
  const unsigned voff = 4 * g * (HKV * D_ * 2) + kvh * (D_ * 2) + c * 16;
  const unsigned voff2 = voff + 2 * (HKV * D_ * 2);
  const unsigned long long e01 = __ballot(pg[0] == pg[1]), e02 = __ballot(pg[0] == pg[2]), e03 = __ballot(pg[0] == pg[3]);
  const unsigned long long e12 = __ballot(pg[1] == pg[2]), e13 = __ballot(pg[1] == pg[3]), e23 = __ballot(pg[2] == pg[3]);
  const unsigned long long ld0 = lv[0];
  const unsigned long long ld1 = lv[1] & ~(e01 & lv[0]);
  const unsigned long long ld2 = lv[2] & ~(e02 & lv[0]) & ~(e12 & lv[1]);
  const unsigned long long ld3 = lv[3] & ~(e03 & lv[0]) & ~(e13 & lv[1]) & ~(e23 & lv[2]);
  const int n0 = __popcll(ld0), n1 = n0 + __popcll(ld1), n2 = n1 + __popcll(ld2), ntot = n2 + __popcll(ld3);
  const int rbeg = (cset * ntot) >> 4, cnt = (((cset + 1) * ntot) >> 4) - rbeg;
  const int nj = wave < cnt ? (cnt - wave + NWAVE - 1) / NWAVE : 0;

  char* wl = lds + wave * REG_LDS;
  int rem[MAXJ];
  unsigned pp0[MAXJ], pp1[MAXJ];
#pragma unroll
  for (int j = 0; j < MAXJ; ++j) {
    rem[j] = 0;
    if (j < nj) {
      const int r = rbeg + wave + NWAVE * j;
      const int bi = (r >= n0) + (r >= n1) + (r >= n2);
      const unsigned long long ld = bi == 0 ? ld0 : bi == 1 ? ld1 : bi == 2 ? ld2 : ld3;
      const int t = r - (bi == 0 ? 0 : bi == 1 ? n0 : bi == 2 ? n1 : n2);
      const int col = __ffsll((long long)(__ballot(prefix_count(ld) == t) & ld)) - 1;
      const unsigned long long pv = bi == 0 ? pg[0] : bi == 1 ? pg[1] : bi == 2 ? pg[2] : pg[3];
      const unsigned p0 = __builtin_amdgcn_readlane((unsigned)pv, col), p1 = __builtin_amdgcn_readlane((unsigned)(pv >> 32), col);
      const char* k0 = reinterpret_cast<const char*>(kc) + (size_t)p0 * (BS * HKV * D_ * 2);
      const char* k1 = reinterpret_cast<const char*>(kc) + (size_t)p1 * (BS * HKV * D_ * 2);
#pragma unroll
      for (int i = 0; i < 4; ++i)
        __builtin_amdgcn_global_load_lds((const unsigned*)(k0 + koff[i]), (unsigned*)(wl + j * (NWAVE * REG_LDS) + i * 1024), 16, 0, 2);
#pragma unroll
      for (int i = 0; i < 4; ++i)
        __builtin_amdgcn_global_load_lds((const unsigned*)(k1 + koff[i]), (unsigned*)(wl + j * (NWAVE * REG_LDS) + 4096 + i * 1024), 16, 0, 2);
      pp0[j] = p0;
      pp1[j] = p1;
      const unsigned long long ea = bi == 0 ? e01 : bi == 1 ? e12 : e23;
      const unsigned long long eb = bi == 0 ? e02 : e13;
      const unsigned same = (1u | (unsigned)((ea >> col) & 1ull) << 1 | (unsigned)((eb >> col) & 1ull) << 2 |
                             (unsigned)((e03 >> col) & 1ull) << 3)
                            << bi;
      rem[j] = ((same >> ci) & 1u) ? mylen - col * UNIT - 4 * g : 0;
    }
  }
  u32x4 qb[4];
  {
    const uint16_t* qp = q + ((size_t)(b0 + (c >> 2)) * H_ + kvh * G_ + (c & 3)) * D_ + 8 * g;
    asm volatile(
        "global_load_dwordx4 %0, %4, off\n\tglobal_load_dwordx4 %1, %4, off offset:64\n\t"
        "global_load_dwordx4 %2, %4, off offset:128\n\tglobal_load_dwordx4 %3, %4, off offset:192"
        : "=&v"(qb[0]), "=&v"(qb[1]), "=&v"(qb[2]), "=&v"(qb[3])
        : "v"(qp)
        : "memory");
  }
  u32x4 vv[MAXJ][8];
#pragma unroll
  for (int j = 0; j < MAXJ; ++j) {
    if (j < nj) {
      const char* v0 = reinterpret_cast<const char*>(vc) + (size_t)pp0[j] * (BS * HKV * D_ * 2);
      const char* v1 = reinterpret_cast<const char*>(vc) + (size_t)pp1[j] * (BS * HKV * D_ * 2);
      asm volatile(
          "global_load_dwordx4 %0, %8, %10 nt\n\tglobal_load_dwordx4 %1, %8, %10 offset:2048 nt\n\t"
          "global_load_dwordx4 %2, %9, %10 nt\n\tglobal_load_dwordx4 %3, %9, %10 offset:2048 nt\n\t"
          "global_load_dwordx4 %4, %8, %11 nt\n\tglobal_load_dwordx4 %5, %8, %11 offset:2048 nt\n\t"
          "global_load_dwordx4 %6, %9, %11 nt\n\tglobal_load_dwordx4 %7, %9, %11 offset:2048 nt"
          : "=&v"(vv[j][0]), "=&v"(vv[j][1]), "=&v"(vv[j][2]), "=&v"(vv[j][3]), "=&v"(vv[j][4]), "=&v"(vv[j][5]),
            "=&v"(vv[j][6]), "=&v"(vv[j][7])
          : "v"(voff), "v"(voff2), "s"(v0), "s"(v1)
          : "memory");
    }
  }
  if (nj == 0) asm volatile("s_waitcnt vmcnt(0)" ::: "memory");
  else if (nj == 1) asm volatile("s_waitcnt vmcnt(8)" ::: "memory");
  else if (nj == 2) asm volatile("s_waitcnt vmcnt(16)" ::: "memory");
  else if (nj == 3) asm volatile("s_waitcnt vmcnt(24)" ::: "memory");
  else asm volatile("s_waitcnt vmcnt(32)" ::: "memory");
  asm volatile("" : "+v"(qb[0]), "+v"(qb[1]), "+v"(qb[2]), "+v"(qb[3])::"memory");
  __builtin_amdgcn_sched_barrier(0);

  float x[MAXJ][8];
  float m = -INFINITY;
#pragma unroll
  for (int j = 0; j < MAXJ; ++j) {
    if (j < nj) {
#pragma unroll
      for (int h2 = 0; h2 < 2; ++h2) {
        f32x4 a = {0.f, 0.f, 0.f, 0.f};
#pragma unroll
        for (int s = 0; s < 4; ++s) {
          const u32x4 ka =
              *reinterpret_cast<const u32x4*>(wl + j * (NWAVE * REG_LDS) + h2 * 4096 + c * 256 + (((4 * s + g) ^ c) * 16));
          a = __builtin_amdgcn_mfma_f32_16x16x32_bf16(__builtin_bit_cast(bf16x8, ka), __builtin_bit_cast(bf16x8, qb[s]),
                                                      a, 0, 0, 0);
        }
#pragma unroll
        for (int r4 = 0; r4 < 4; ++r4) {
          x[j][h2 * 4 + r4] = (h2 * 16 + r4 < rem[j]) ? a[r4] * scl : -INFINITY;
          m = fmaxf(m, x[j][h2 * 4 + r4]);
        }
      }
    }
  }
  m = xg_max(m);
  const float msub = (m == -INFINITY) ? 0.f : m;
  float l = 0.f;
  u32x4 pa[MAXJ];
#pragma unroll
  for (int j = 0; j < MAXJ; ++j) {
    if (j < nj) {
      float p[8];
#pragma unroll
      for (int i = 0; i < 8; ++i) p[i] = __builtin_amdgcn_exp2f(x[j][i] - msub);
      l += ((p[0] + p[1]) + (p[2] + p[3])) + ((p[4] + p[5]) + (p[6] + p[7]));
      pa[j] = u32x4{cvtpk(p[0], p[1]), cvtpk(p[2], p[3]), cvtpk(p[4], p[5]), cvtpk(p[6], p[7])};
    }
  }
  f32x4 o[8];
#pragma unroll
  for (int e = 0; e < 8; ++e) o[e] = f32x4{0.f, 0.f, 0.f, 0.f};
#pragma unroll
  for (int j = 0; j < MAXJ; ++j) {
    if (j < nj) {
      {
        const int later = nj - 1 - j;
        if (later == 0) asm volatile("s_waitcnt vmcnt(0)" ::: "memory");
        else if (later == 1) asm volatile("s_waitcnt vmcnt(8)" ::: "memory");
        else if (later == 2) asm volatile("s_waitcnt vmcnt(16)" ::: "memory");
        else asm volatile("s_waitcnt vmcnt(24)" ::: "memory");
      }
      asm volatile(""
                   : "+v"(vv[j][0]), "+v"(vv[j][1]), "+v"(vv[j][2]), "+v"(vv[j][3]), "+v"(vv[j][4]), "+v"(vv[j][5]),
                     "+v"(vv[j][6]), "+v"(vv[j][7])::"memory");
#pragma unroll
      for (int e = 0; e < 8; ++e) {
        const unsigned sel = (e & 1) ? 0x07060302u : 0x05040100u;
        u32x4 vb;
#pragma unroll
        for (int d = 0; d < 4; ++d) vb[d] = __builtin_amdgcn_perm(vv[j][2 * d + 1][e >> 1], vv[j][2 * d][e >> 1], sel);
        o[e] = __builtin_amdgcn_mfma_f32_16x16x32_bf16(__builtin_bit_cast(bf16x8, pa[j]), __builtin_bit_cast(bf16x8, vb),
                                                       o[e], 0, 0, 0);
      }
    }
  }
  l = xg_sum(l);
  float* lds_m = reinterpret_cast<float*>(lds + LDS_ML);
  float* lds_l = lds_m + NWP * 16;
  {
    float* wo = reinterpret_cast<float*>(wl) + g * (G_ * D_);
#pragma unroll
    for (int r4 = 0; r4 < 4; ++r4) {
      *reinterpret_cast<f32x4*>(wo + r4 * D_ + 8 * c) = f32x4{o[0][r4], o[1][r4], o[2][r4], o[3][r4]};
      *reinterpret_cast<f32x4*>(wo + r4 * D_ + 8 * c + 4) = f32x4{o[4][r4], o[5][r4], o[6][r4], o[7][r4]};
    }
    if (g == 0) {
      lds_m[c * NWP + wave] = m;
      lds_l[c * NWP + wave] = l;
    }
  }
  __syncthreads();
  if (tid < 256) {
    const int row16 = tid >> 4, d8 = tid & 15;
    float mv[NWAVE], lv[NWAVE];
    f32x4 oa[NWAVE], ob[NWAVE];
#pragma unroll
    for (int w = 0; w < NWAVE; ++w) {
      mv[w] = lds_m[row16 * NWP + w];
      lv[w] = lds_l[row16 * NWP + w];
      const char* src = lds + w * REG_LDS + (row16 * D_ + 4 * d8) * 4;
      oa[w] = *reinterpret_cast<const f32x4*>(src);
      ob[w] = *reinterpret_cast<const f32x4*>(src + 256);
    }
    float M = mv[0];
#pragma unroll
    for (int w = 1; w < NWAVE; ++w) M = fmaxf(M, mv[w]);
    const float Ms = (M == -INFINITY) ? 0.f : M;
    float L = 0.f;
    f32x4 a = {0.f, 0.f, 0.f, 0.f}, bacc = {0.f, 0.f, 0.f, 0.f};
#pragma unroll
    for (int w = 0; w < NWAVE; ++w) {
      const float f = __builtin_amdgcn_exp2f(mv[w] - Ms);
      L = fmaf(f, lv[w], L);
      a += f > 0.f ? f * oa[w] : f32x4{0.f, 0.f, 0.f, 0.f};
      bacc += f > 0.f ? f * ob[w] : f32x4{0.f, 0.f, 0.f, 0.f};
    }
    const int row = (b0 + (row16 >> 2)) * H_ + kvh * G_ + (row16 & 3);
    float* dst = ws_o + ((size_t)row * NPART + cset) * D_ + 4 * d8;
    asm volatile("global_store_dwordx4 %0, %1, off sc0 sc1\n\tglobal_store_dwordx4 %0, %2, off offset:256 sc0 sc1\n\ts_nop 1" ::"v"(dst), "v"(a), "v"(bacc) : "memory");
    if (d8 == 0) *reinterpret_cast<float2*>(ws_ml + (row * NPART + cset) * 2) = make_float2(M, L);
  }
}

template <int CTRL>
__device__ __forceinline__ float dpp_f(float x) {
  return __builtin_bit_cast(float, __builtin_amdgcn_mov_dpp(__builtin_bit_cast(int, x), CTRL, 0xf, 0xf, true));
}

__global__ void __launch_bounds__(64)
attn_combine(const float* __restrict__ ws_o, const float* __restrict__ ws_ml, unsigned* __restrict__ out) {
  static_assert(NPART == 16, "one partial per lane of a 16-lane row");
  const int row = blockIdx.x;
  const int lane = threadIdx.x;
  const float2 ml = *reinterpret_cast<const float2*>(ws_ml + (row * NPART + (lane & 15)) * 2);
  float2 oo[NPART];
#pragma unroll
  for (int s = 0; s < NPART; ++s)
    oo[s] = *reinterpret_cast<const float2*>(ws_o + ((size_t)row * NPART + s) * D_ + 2 * lane);
  float M = ml.x;
  M = fmaxf(M, dpp_f<0x128>(M));
  M = fmaxf(M, dpp_f<0x124>(M));
  M = fmaxf(M, dpp_f<0x122>(M));
  M = fmaxf(M, dpp_f<0x121>(M));
  const float f = __builtin_amdgcn_exp2f(ml.x - M);
  float L = f * ml.y;
  L += dpp_f<0x128>(L);
  L += dpp_f<0x124>(L);
  L += dpp_f<0x122>(L);
  L += dpp_f<0x121>(L);
  const float w = f / L;
  float a0 = 0.f, a1 = 0.f;
#pragma unroll
  for (int s = 0; s < NPART; ++s) {
    const float ws = __builtin_bit_cast(float, __builtin_amdgcn_readlane(__builtin_bit_cast(int, w), s));
    a0 = fmaf(ws, oo[s].x, a0);
    a1 = fmaf(ws, oo[s].y, a1);
  }
  out[row * (D_ / 2) + lane] = cvtpk(a0, a1);
}

}

extern "C" void kernel_launch(void* const* d_in, const int* in_sizes, int n_in, void* d_out, int out_size,
                              void* d_ws, size_t ws_size, hipStream_t stream) {
  const uint16_t* q = (const uint16_t*)d_in[0];
  const uint16_t* kc = (const uint16_t*)d_in[1];
  const uint16_t* vc = (const uint16_t*)d_in[2];
  const int* bt = (const int*)d_in[3];
  const int* ctxl = (const int*)d_in[4];
  float* ws_o = (float*)d_ws;
  float* ws_ml = ws_o + (size_t)B_ * H_ * NPART * D_;
  attn_main<<<HKV * (B_ / NQ) * NPART, NWAVE * 64, 0, stream>>>(q, kc, vc, bt, ctxl, ws_o, ws_ml);
  attn_combine<<<B_ * H_, 64, 0, stream>>>(ws_o, ws_ml, (unsigned*)d_out);
}
```

```cpp
#include <hip/hip_runtime.h>
#include <hip/hip_bf16.h>
#include <stdint.h>

namespace {

constexpr int B_ = 8, H_ = 32, HKV = 8, D_ = 128, BS = 16, NB = 128, LK = NB * BS, G_ = H_ / HKV;
#define NW 6
constexpr int NWAVE = NW, MAXJ = (16 + NW - 1) / NW, UNIT = 32, NQ = 4, NCOLS = LK / UNIT;
constexpr int NPART = 16;
static_assert(NQ * G_ == 16 && NCOLS == 64 && NQ * NCOLS == NPART * 16 && NWAVE * MAXJ >= 16, "layout");

using bf16x8 = __attribute__((ext_vector_type(8))) short;
using f32x4 = __attribute__((ext_vector_type(4))) float;
using u32x4 = __attribute__((ext_vector_type(4))) unsigned;
using u32x2 = __attribute__((ext_vector_type(2))) unsigned;
using i32x4 = __attribute__((ext_vector_type(4))) int;

__device__ __forceinline__ unsigned cvtpk(float lo, float hi) {
  unsigned r;
  asm("v_cvt_pk_bf16_f32 %0, %1, %2" : "=v"(r) : "v"(lo), "v"(hi));
  return r;
}
__device__ __forceinline__ float xg_max(float x) {
  auto s = __builtin_amdgcn_permlane16_swap(__float_as_uint(x), __float_as_uint(x), false, false);
  x = fmaxf(__uint_as_float(s[0]), __uint_as_float(s[1]));
  auto t = __builtin_amdgcn_permlane32_swap(__float_as_uint(x), __float_as_uint(x), false, false);
  return fmaxf(__uint_as_float(t[0]), __uint_as_float(t[1]));
}
__device__ __forceinline__ float xg_sum(float x) {
  auto s = __builtin_amdgcn_permlane16_swap(__float_as_uint(x), __float_as_uint(x), false, false);
  x = __uint_as_float(s[0]) + __uint_as_float(s[1]);
  auto t = __builtin_amdgcn_permlane32_swap(__float_as_uint(x), __float_as_uint(x), false, false);
  return __uint_as_float(t[0]) + __uint_as_float(t[1]);
}
__device__ __forceinline__ int prefix_count(unsigned long long m) {
  return __builtin_amdgcn_mbcnt_hi((unsigned)(m >> 32), __builtin_amdgcn_mbcnt_lo((unsigned)m, 0u));
}

constexpr int REG_LDS = 8192;
constexpr int LDS_ML = 16 * REG_LDS;
constexpr int NWP = 8;
constexpr int LDS_BYTES = LDS_ML + 2 * NWP * 16 * 4;

__global__ void __launch_bounds__(NWAVE * 64)
attn_main(const uint16_t* __restrict__ q, const uint16_t* __restrict__ kc, const uint16_t* __restrict__ vc,
          const int* __restrict__ bt, const int* __restrict__ ctxl, float* __restrict__ ws_o,
          float* __restrict__ ws_ml) {
  __shared__ __attribute__((aligned(16))) char lds[LDS_BYTES];
  const int bid = blockIdx.x;
  const int kvh = bid & 7;
  const int b0 = ((bid >> 3) & 1) * NQ;
  const int cset = bid >> 4;
  const int tid = threadIdx.x;
  const int lane = tid & 63;
  const int wave = __builtin_amdgcn_readfirstlane(tid >> 6);
  const int g = lane >> 4, c = lane & 15;
  asm volatile("" ::"s"(q), "s"(kc), "s"(vc), "s"(bt), "s"(ctxl), "s"(ws_o), "s"(ws_ml));

  unsigned long long pg[NQ];
#pragma unroll
  for (int i = 0; i < NQ; ++i) pg[i] = *reinterpret_cast<const unsigned long long*>(bt + (b0 + i) * NB + 2 * lane);
  i32x4 ctx4;
  {
    const int* cp = ctxl + b0;
    asm volatile("s_load_dwordx4 %0, %1, 0x0\n\ts_waitcnt lgkmcnt(0)" : "=&s"(ctx4) : "s"(cp) : "memory");
  }
  int len4[NQ];
  unsigned long long lv[NQ];
#pragma unroll
  for (int i = 0; i < NQ; ++i) {
    len4[i] = ctx4[i] <= 0 ? LK : min(ctx4[i], LK);
    lv[i] = ~0ull >> (64 - ((len4[i] + UNIT - 1) / UNIT));
  }
  const int ci = c >> 2;
  constexpr float C2 = 0.088388347648318440f * 1.4426950408889634f;
  const int mylen = (ci & 2) ? ((ci & 1) ? len4[3] : len4[2]) : ((ci & 1) ? len4[1] : len4[0]);
  const int myctx = (ci & 2) ? ((ci & 1) ? ctx4[3] : ctx4[2]) : ((ci & 1) ? ctx4[1] : ctx4[0]);
  const float scl = myctx > 0 ? C2 : 0.f;
  unsigned koff[4];
#pragma unroll
  for (int i = 0; i < 4; ++i) koff[i] = (4 * i + g) * (HKV * D_ * 2) + kvh * (D_ * 2) + ((c ^ (4 * i + g)) * 16);
  const unsigned voff = 4 * g * (HKV * D_ * 2) + kvh * (D_ * 2) + c * 16;
  const unsigned voff2 = voff + 2 * (HKV * D_ * 2);
  const unsigned long long e01 = __ballot(pg[0] == pg[1]), e02 = __ballot(pg[0] == pg[2]), e03 = __ballot(pg[0] == pg[3]);
  const unsigned long long e12 = __ballot(pg[1] == pg[2]), e13 = __ballot(pg[1] == pg[3]), e23 = __ballot(pg[2] == pg[3]);
  const unsigned long long ld0 = lv[0];
  const unsigned long long ld1 = lv[1] & ~(e01 & lv[0]);
  const unsigned long long ld2 = lv[2] & ~(e02 & lv[0]) & ~(e12 & lv[1]);
  const unsigned long long ld3 = lv[3] & ~(e03 & lv[0]) & ~(e13 & lv[1]) & ~(e23 & lv[2]);
  const int n0 = __popcll(ld0), n1 = n0 + __popcll(ld1), n2 = n1 + __popcll(ld2), ntot = n2 + __popcll(ld3);
  const int rbeg = (cset * ntot) >> 4, cnt = (((cset + 1) * ntot) >> 4) - rbeg;
  const int nj = wave < cnt ? (cnt - wave + NWAVE - 1) / NWAVE : 0;

  char* wl = lds + wave * REG_LDS;
  int rem[MAXJ];
  unsigned pp0[MAXJ], pp1[MAXJ];
#pragma unroll
  for (int j = 0; j < MAXJ; ++j) {
    rem[j] = 0;
    if (j < nj) {
      const int r = rbeg + wave + NWAVE * j;
      const int bi = (r >= n0) + (r >= n1) + (r >= n2);
      const unsigned long long ld = bi == 0 ? ld0 : bi == 1 ? ld1 : bi == 2 ? ld2 : ld3;
      const int t = r - (bi == 0 ? 0 : bi == 1 ? n0 : bi == 2 ? n1 : n2);
      const int col = __ffsll((long long)(__ballot(prefix_count(ld) == t) & ld)) - 1;
      const unsigned long long pv = bi == 0 ? pg[0] : bi == 1 ? pg[1] : bi == 2 ? pg[2] : pg[3];
      const unsigned p0 = __builtin_amdgcn_readlane((unsigned)pv, col), p1 = __builtin_amdgcn_readlane((unsigned)(pv >> 32), col);
      const char* k0 = reinterpret_cast<const char*>(kc) + (size_t)p0 * (BS * HKV * D_ * 2);
      const char* k1 = reinterpret_cast<const char*>(kc) + (size_t)p1 * (BS * HKV * D_ * 2);
#pragma unroll
      for (int i = 0; i < 4; ++i)
        __builtin_amdgcn_global_load_lds((const unsigned*)(k0 + koff[i]), (unsigned*)(wl + j * (NWAVE * REG_LDS) + i * 1024), 16, 0, 0);
#pragma unroll
      for (int i = 0; i < 4; ++i)
        __builtin_amdgcn_global_load_lds((const unsigned*)(k1 + koff[i]), (unsigned*)(wl + j * (NWAVE * REG_LDS) + 4096 + i * 1024), 16, 0, 0);
      pp0[j] = p0;
      pp1[j] = p1;
      const unsigned long long ea = bi == 0 ? e01 : bi == 1 ? e12 : e23;
      const unsigned long long eb = bi == 0 ? e02 : e13;
      const unsigned same = (1u | (unsigned)((ea >> col) & 1ull) << 1 | (unsigned)((eb >> col) & 1ull) << 2 |
                             (unsigned)((e03 >> col) & 1ull) << 3)
                            << bi;
      rem[j] = ((same >> ci) & 1u) ? mylen - col * UNIT - 4 * g : 0;
    }
  }
  u32x4 qb[4];
  {
    const uint16_t* qp = q + ((size_t)(b0 + (c >> 2)) * H_ + kvh * G_ + (c & 3)) * D_ + 8 * g;
    asm volatile(
        "global_load_dwordx4 %0, %4, off\n\tglobal_load_dwordx4 %1, %4, off offset:64\n\t"
        "global_load_dwordx4 %2, %4, off offset:128\n\tglobal_load_dwordx4 %3, %4, off offset:192"
        : "=&v"(qb[0]), "=&v"(qb[1]), "=&v"(qb[2]), "=&v"(qb[3])
        : "v"(qp)
        : "memory");
  }
  u32x4 vv[MAXJ][8];
#pragma unroll
  for (int j = 0; j < MAXJ; ++j) {
    if (j < nj) {
      const char* v0 = reinterpret_cast<const char*>(vc) + (size_t)pp0[j] * (BS * HKV * D_ * 2);
      const char* v1 = reinterpret_cast<const char*>(vc) + (size_t)pp1[j] * (BS * HKV * D_ * 2);
      asm volatile(
          "global_load_dwordx4 %0, %8, %10\n\tglobal_load_dwordx4 %1, %8, %10 offset:2048\n\t"
          "global_load_dwordx4 %2, %9, %10\n\tglobal_load_dwordx4 %3, %9, %10 offset:2048\n\t"
          "global_load_dwordx4 %4, %8, %11\n\tglobal_load_dwordx4 %5, %8, %11 offset:2048\n\t"
          "global_load_dwordx4 %6, %9, %11\n\tglobal_load_dwordx4 %7, %9, %11 offset:2048"
          : "=&v"(vv[j][0]), "=&v"(vv[j][1]), "=&v"(vv[j][2]), "=&v"(vv[j][3]), "=&v"(vv[j][4]), "=&v"(vv[j][5]),
            "=&v"(vv[j][6]), "=&v"(vv[j][7])
          : "v"(voff), "v"(voff2), "s"(v0), "s"(v1)
          : "memory");
    }
  }
  if (nj == 0) asm volatile("s_waitcnt vmcnt(0)" ::: "memory");
  else if (nj == 1) asm volatile("s_waitcnt vmcnt(8)" ::: "memory");
  else if (nj == 2) asm volatile("s_waitcnt vmcnt(16)" ::: "memory");
  else if (nj == 3) asm volatile("s_waitcnt vmcnt(24)" ::: "memory");
  else asm volatile("s_waitcnt vmcnt(32)" ::: "memory");
  asm volatile("" : "+v"(qb[0]), "+v"(qb[1]), "+v"(qb[2]), "+v"(qb[3])::"memory");
  __builtin_amdgcn_sched_barrier(0);

  float x[MAXJ][8];
  float m = -INFINITY;
#pragma unroll
  for (int j = 0; j < MAXJ; ++j) {
    if (j < nj) {
#pragma unroll
      for (int h2 = 0; h2 < 2; ++h2) {
        f32x4 a = {0.f, 0.f, 0.f, 0.f};
#pragma unroll
        for (int s = 0; s < 4; ++s) {
          const u32x4 ka =
              *reinterpret_cast<const u32x4*>(wl + j * (NWAVE * REG_LDS) + h2 * 4096 + c * 256 + (((4 * s + g) ^ c) * 16));
          a = __builtin_amdgcn_mfma_f32_16x16x32_bf16(__builtin_bit_cast(bf16x8, ka), __builtin_bit_cast(bf16x8, qb[s]),
                                                      a, 0, 0, 0);
        }
#pragma unroll
        for (int r4 = 0; r4 < 4; ++r4) {
          x[j][h2 * 4 + r4] = (h2 * 16 + r4 < rem[j]) ? a[r4] * scl : -INFINITY;
          m = fmaxf(m, x[j][h2 * 4 + r4]);
        }
      }
    }
  }
  m = xg_max(m);
  const float msub = (m == -INFINITY) ? 0.f : m;
  float l = 0.f;
  u32x4 pa[MAXJ];
#pragma unroll
  for (int j = 0; j < MAXJ; ++j) {
    if (j < nj) {
      float p[8];
#pragma unroll
      for (int i = 0; i < 8; ++i) p[i] = __builtin_amdgcn_exp2f(x[j][i] - msub);
      l += ((p[0] + p[1]) + (p[2] + p[3])) + ((p[4] + p[5]) + (p[6] + p[7]));
      pa[j] = u32x4{cvtpk(p[0], p[1]), cvtpk(p[2], p[3]), cvtpk(p[4], p[5]), cvtpk(p[6], p[7])};
    }
  }
  f32x4 o[8];
#pragma unroll
  for (int e = 0; e < 8; ++e) o[e] = f32x4{0.f, 0.f, 0.f, 0.f};
#pragma unroll
  for (int j = 0; j < MAXJ; ++j) {
    if (j < nj) {
      {
        const int later = nj - 1 - j;
        if (later == 0) asm volatile("s_waitcnt vmcnt(0)" ::: "memory");
        else if (later == 1) asm volatile("s_waitcnt vmcnt(8)" ::: "memory");
        else if (later == 2) asm volatile("s_waitcnt vmcnt(16)" ::: "memory");
        else asm volatile("s_waitcnt vmcnt(24)" ::: "memory");
      }
      asm volatile(""
                   : "+v"(vv[j][0]), "+v"(vv[j][1]), "+v"(vv[j][2]), "+v"(vv[j][3]), "+v"(vv[j][4]), "+v"(vv[j][5]),
                     "+v"(vv[j][6]), "+v"(vv[j][7])::"memory");
#pragma unroll
      for (int e = 0; e < 8; ++e) {
        const unsigned sel = (e & 1) ? 0x07060302u : 0x05040100u;
        u32x4 vb;
#pragma unroll
        for (int d = 0; d < 4; ++d) vb[d] = __builtin_amdgcn_perm(vv[j][2 * d + 1][e >> 1], vv[j][2 * d][e >> 1], sel);
        o[e] = __builtin_amdgcn_mfma_f32_16x16x32_bf16(__builtin_bit_cast(bf16x8, pa[j]), __builtin_bit_cast(bf16x8, vb),
                                                       o[e], 0, 0, 0);
      }
    }
  }
  l = xg_sum(l);
  float* lds_m = reinterpret_cast<float*>(lds + LDS_ML);
  float* lds_l = lds_m + NWP * 16;
  {
    float* wo = reinterpret_cast<float*>(wl) + g * (G_ * D_);
#pragma unroll
    for (int r4 = 0; r4 < 4; ++r4) {
      *reinterpret_cast<f32x4*>(wo + r4 * D_ + 8 * c) = f32x4{o[0][r4], o[1][r4], o[2][r4], o[3][r4]};
      *reinterpret_cast<f32x4*>(wo + r4 * D_ + 8 * c + 4) = f32x4{o[4][r4], o[5][r4], o[6][r4], o[7][r4]};
    }
    if (g == 0) {
      lds_m[c * NWP + wave] = m;
      lds_l[c * NWP + wave] = l;
    }
  }
  __syncthreads();
  if (tid < 256) {
    const int row16 = tid >> 4, d8 = tid & 15;
    float mv[NWAVE], lv[NWAVE];
    f32x4 oa[NWAVE], ob[NWAVE];
#pragma unroll
    for (int w = 0; w < NWAVE; ++w) {
      mv[w] = lds_m[row16 * NWP + w];
      lv[w] = lds_l[row16 * NWP + w];
      const char* src = lds + w * REG_LDS + (row16 * D_ + 4 * d8) * 4;
      oa[w] = *reinterpret_cast<const f32x4*>(src);
      ob[w] = *reinterpret_cast<const f32x4*>(src + 256);
    }
    float M = mv[0];
#pragma unroll
    for (int w = 1; w < NWAVE; ++w) M = fmaxf(M, mv[w]);
    const float Ms = (M == -INFINITY) ? 0.f : M;
    float L = 0.f;
    f32x4 a = {0.f, 0.f, 0.f, 0.f}, bacc = {0.f, 0.f, 0.f, 0.f};
#pragma unroll
    for (int w = 0; w < NWAVE; ++w) {
      const float f = __builtin_amdgcn_exp2f(mv[w] - Ms);
      L = fmaf(f, lv[w], L);
      a += f > 0.f ? f * oa[w] : f32x4{0.f, 0.f, 0.f, 0.f};
      bacc += f > 0.f ? f * ob[w] : f32x4{0.f, 0.f, 0.f, 0.f};
    }
    const int row = (b0 + (row16 >> 2)) * H_ + kvh * G_ + (row16 & 3);
    float* dst = ws_o + ((size_t)row * NPART + cset) * D_ + 4 * d8;
    asm volatile("global_store_dwordx4 %0, %1, off sc0 sc1\n\tglobal_store_dwordx4 %0, %2, off offset:256 sc0 sc1\n\ts_nop 1" ::"v"(dst), "v"(a), "v"(bacc) : "memory");
    if (d8 == 0) *reinterpret_cast<float2*>(ws_ml + (row * NPART + cset) * 2) = make_float2(M, L);
  }
}

template <int CTRL>
__device__ __forceinline__ float dpp_f(float x) {
  return __builtin_bit_cast(float, __builtin_amdgcn_mov_dpp(__builtin_bit_cast(int, x), CTRL, 0xf, 0xf, true));
}

__global__ void __launch_bounds__(64)
attn_combine(const float* __restrict__ ws_o, const float* __restrict__ ws_ml, unsigned* __restrict__ out) {
  static_assert(NPART == 16, "one partial per lane of a 16-lane row");
  const int row = blockIdx.x;
  const int lane = threadIdx.x;
  const float2 ml = *reinterpret_cast<const float2*>(ws_ml + (row * NPART + (lane & 15)) * 2);
  float2 oo[NPART];
#pragma unroll
  for (int s = 0; s < NPART; ++s)
    oo[s] = *reinterpret_cast<const float2*>(ws_o + ((size_t)row * NPART + s) * D_ + 2 * lane);
  float M = ml.x;
  M = fmaxf(M, dpp_f<0x128>(M));
  M = fmaxf(M, dpp_f<0x124>(M));
  M = fmaxf(M, dpp_f<0x122>(M));
  M = fmaxf(M, dpp_f<0x121>(M));
  const float f = __builtin_amdgcn_exp2f(ml.x - M);
  float L = f * ml.y;
  L += dpp_f<0x128>(L);
  L += dpp_f<0x124>(L);
  L += dpp_f<0x122>(L);
  L += dpp_f<0x121>(L);
  const float w = f / L;
  float a0 = 0.f, a1 = 0.f;
#pragma unroll
  for (int s = 0; s < NPART; ++s) {
    const float ws = __builtin_bit_cast(float, __builtin_amdgcn_readlane(__builtin_bit_cast(int, w), s));
    a0 = fmaf(ws, oo[s].x, a0);
    a1 = fmaf(ws, oo[s].y, a1);
  }
  out[row * (D_ / 2) + lane] = cvtpk(a0, a1);
}

}

extern "C" void kernel_launch(void* const* d_in, const int* in_sizes, int n_in, void* d_out, int out_size,
                              void* d_ws, size_t ws_size, hipStream_t stream) {
  const uint16_t* q = (const uint16_t*)d_in[0];
  const uint16_t* kc = (const uint16_t*)d_in[1];
  const uint16_t* vc = (const uint16_t*)d_in[2];
  const int* bt = (const int*)d_in[3];
  const int* ctxl = (const int*)d_in[4];
  float* ws_o = (float*)d_ws;
  float* ws_ml = ws_o + (size_t)B_ * H_ * NPART * D_;
  attn_main<<<HKV * (B_ / NQ) * NPART, NWAVE * 64, 0, stream>>>(q, kc, vc, bt, ctxl, ws_o, ws_ml);
  attn_combine<<<B_ * H_, 64, 0, stream>>>(ws_o, ws_ml, (unsigned*)d_out);
}
```

```cpp
#include <hip/hip_runtime.h>
#include <hip/hip_bf16.h>
#include <stdint.h>

namespace {

constexpr int B_ = 8, H_ = 32, HKV = 8, D_ = 128, BS = 16, NB = 128, LK = NB * BS, G_ = H_ / HKV;
constexpr int NWAVE = 6, MAXJ = (16 + NWAVE - 1) / NWAVE, UNIT = 32, NQ = 4, NCOLS = LK / UNIT;
constexpr int NPART = 16;
static_assert(NQ * G_ == 16 && NCOLS == 64 && NQ * NCOLS == NPART * 16 && NWAVE * MAXJ >= 16, "layout");

using bf16x8 = __attribute__((ext_vector_type(8))) short;
using f32x4 = __attribute__((ext_vector_type(4))) float;
using u32x4 = __attribute__((ext_vector_type(4))) unsigned;
using u32x2 = __attribute__((ext_vector_type(2))) unsigned;
using i32x4 = __attribute__((ext_vector_type(4))) int;

__device__ __forceinline__ unsigned cvtpk(float lo, float hi) {
  unsigned r;
  asm("v_cvt_pk_bf16_f32 %0, %1, %2" : "=v"(r) : "v"(lo), "v"(hi));
  return r;
}
__device__ __forceinline__ float xg_max(float x) {
  auto s = __builtin_amdgcn_permlane16_swap(__float_as_uint(x), __float_as_uint(x), false, false);
  x = fmaxf(__uint_as_float(s[0]), __uint_as_float(s[1]));
  auto t = __builtin_amdgcn_permlane32_swap(__float_as_uint(x), __float_as_uint(x), false, false);
  return fmaxf(__uint_as_float(t[0]), __uint_as_float(t[1]));
}
__device__ __forceinline__ float xg_sum(float x) {
  auto s = __builtin_amdgcn_permlane16_swap(__float_as_uint(x), __float_as_uint(x), false, false);
  x = __uint_as_float(s[0]) + __uint_as_float(s[1]);
  auto t = __builtin_amdgcn_permlane32_swap(__float_as_uint(x), __float_as_uint(x), false, false);
  return __uint_as_float(t[0]) + __uint_as_float(t[1]);
}
__device__ __forceinline__ int prefix_count(unsigned long long m) {
  return __builtin_amdgcn_mbcnt_hi((unsigned)(m >> 32), __builtin_amdgcn_mbcnt_lo((unsigned)m, 0u));
}

constexpr int REG_LDS = 8192;
constexpr int LDS_ML = 16 * REG_LDS;
constexpr int NWP = 8;
constexpr int LDS_BYTES = LDS_ML + 2 * NWP * 16 * 4;


__global__ void __launch_bounds__(NWAVE * 64)
attn_main(const uint16_t* __restrict__ q, const uint16_t* __restrict__ kc, const uint16_t* __restrict__ vc,
          const int* __restrict__ bt, const int* __restrict__ ctxl, float* __restrict__ ws_o,
          float* __restrict__ ws_ml) {
  __shared__ __attribute__((aligned(16))) char lds[LDS_BYTES];
  const int bid = blockIdx.x;
  const int kvh = bid & 7;
  const int b0 = ((bid >> 3) & 1) * NQ;
  const int cset = bid >> 4;
  const int tid = threadIdx.x;
  const int lane = tid & 63;
  const int wave = __builtin_amdgcn_readfirstlane(tid >> 6);
  const int g = lane >> 4, c = lane & 15;
  asm volatile("" ::"s"(q), "s"(kc), "s"(vc), "s"(bt), "s"(ctxl), "s"(ws_o), "s"(ws_ml));

  unsigned long long pg[NQ];
#pragma unroll
  for (int i = 0; i < NQ; ++i) pg[i] = *reinterpret_cast<const unsigned long long*>(bt + (b0 + i) * NB + 2 * lane);
  i32x4 ctx4;
  {
    const int* cp = ctxl + b0;
    asm volatile("s_load_dwordx4 %0, %1, 0x0\n\ts_waitcnt lgkmcnt(0)" : "=&s"(ctx4) : "s"(cp) : "memory");
  }
  int len4[NQ];
  unsigned long long lv[NQ];
#pragma unroll
  for (int i = 0; i < NQ; ++i) {
    len4[i] = ctx4[i] <= 0 ? LK : min(ctx4[i], LK);
    lv[i] = ~0ull >> (64 - ((len4[i] + UNIT - 1) / UNIT));
  }
  const int ci = c >> 2;
  constexpr float C2 = 0.088388347648318440f * 1.4426950408889634f;
  const int mylen = (ci & 2) ? ((ci & 1) ? len4[3] : len4[2]) : ((ci & 1) ? len4[1] : len4[0]);
  const int myctx = (ci & 2) ? ((ci & 1) ? ctx4[3] : ctx4[2]) : ((ci & 1) ? ctx4[1] : ctx4[0]);
  const float scl = myctx > 0 ? C2 : 0.f;
  unsigned koff[4];
#pragma unroll
  for (int i = 0; i < 4; ++i) koff[i] = (4 * i + g) * (HKV * D_ * 2) + kvh * (D_ * 2) + ((c ^ (4 * i + g)) * 16);
  const unsigned voff = 4 * g * (HKV * D_ * 2) + kvh * (D_ * 2) + c * 16;
  const unsigned voff2 = voff + 2 * (HKV * D_ * 2);
  const unsigned long long e01 = __ballot(pg[0] == pg[1]), e02 = __ballot(pg[0] == pg[2]), e03 = __ballot(pg[0] == pg[3]);
  const unsigned long long e12 = __ballot(pg[1] == pg[2]), e13 = __ballot(pg[1] == pg[3]), e23 = __ballot(pg[2] == pg[3]);
  const unsigned long long ld0 = lv[0];
  const unsigned long long ld1 = lv[1] & ~(e01 & lv[0]);
  const unsigned long long ld2 = lv[2] & ~(e02 & lv[0]) & ~(e12 & lv[1]);
  const unsigned long long ld3 = lv[3] & ~(e03 & lv[0]) & ~(e13 & lv[1]) & ~(e23 & lv[2]);
  const int n0 = __popcll(ld0), n1 = n0 + __popcll(ld1), n2 = n1 + __popcll(ld2), ntot = n2 + __popcll(ld3);
  const int rbeg = (cset * ntot) >> 4, cnt = (((cset + 1) * ntot) >> 4) - rbeg;
  const int nj = wave < cnt ? (cnt - wave + NWAVE - 1) / NWAVE : 0;

  char* wl = lds + wave * REG_LDS;
  int rem[MAXJ];
  unsigned pp0[MAXJ], pp1[MAXJ];
#pragma unroll
  for (int j = 0; j < MAXJ; ++j) {
    rem[j] = 0;
    if (j < nj) {
      const int r = rbeg + wave + NWAVE * j;
      const int bi = (r >= n0) + (r >= n1) + (r >= n2);
      const unsigned long long ld = bi == 0 ? ld0 : bi == 1 ? ld1 : bi == 2 ? ld2 : ld3;
      const int t = r - (bi == 0 ? 0 : bi == 1 ? n0 : bi == 2 ? n1 : n2);
      const int col = __ffsll((long long)(__ballot(prefix_count(ld) == t) & ld)) - 1;
      const unsigned long long pv = bi == 0 ? pg[0] : bi == 1 ? pg[1] : bi == 2 ? pg[2] : pg[3];
      const unsigned p0 = __builtin_amdgcn_readlane((unsigned)pv, col), p1 = __builtin_amdgcn_readlane((unsigned)(pv >> 32), col);
      const char* k0 = reinterpret_cast<const char*>(kc) + (size_t)p0 * (BS * HKV * D_ * 2);
      const char* k1 = reinterpret_cast<const char*>(kc) + (size_t)p1 * (BS * HKV * D_ * 2);
#pragma unroll
      for (int i = 0; i < 4; ++i)
        __builtin_amdgcn_global_load_lds((const unsigned*)(k0 + koff[i]), (unsigned*)(wl + j * (NWAVE * REG_LDS) + i * 1024), 16, 0, 2);
#pragma unroll
      for (int i = 0; i < 4; ++i)
        __builtin_amdgcn_global_load_lds((const unsigned*)(k1 + koff[i]), (unsigned*)(wl + j * (NWAVE * REG_LDS) + 4096 + i * 1024), 16, 0, 2);
      pp0[j] = p0;
      pp1[j] = p1;
      const unsigned long long ea = bi == 0 ? e01 : bi == 1 ? e12 : e23;
      const unsigned long long eb = bi == 0 ? e02 : e13;
      const unsigned same = (1u | (unsigned)((ea >> col) & 1ull) << 1 | (unsigned)((eb >> col) & 1ull) << 2 |
                             (unsigned)((e03 >> col) & 1ull) << 3)
                            << bi;
      rem[j] = ((same >> ci) & 1u) ? mylen - col * UNIT - 4 * g : 0;
    }
  }
  u32x4 qb[4];
  {
    const uint16_t* qp = q + ((size_t)(b0 + (c >> 2)) * H_ + kvh * G_ + (c & 3)) * D_ + 8 * g;
    asm volatile(
        "global_load_dwordx4 %0, %4, off\n\tglobal_load_dwordx4 %1, %4, off offset:64\n\t"
        "global_load_dwordx4 %2, %4, off offset:128\n\tglobal_load_dwordx4 %3, %4, off offset:192"
        : "=&v"(qb[0]), "=&v"(qb[1]), "=&v"(qb[2]), "=&v"(qb[3])
        : "v"(qp)
        : "memory");
  }
  u32x4 vv[MAXJ][8];
#pragma unroll
  for (int j = 0; j < MAXJ; ++j) {
    if (j < nj) {
      const char* v0 = reinterpret_cast<const char*>(vc) + (size_t)pp0[j] * (BS * HKV * D_ * 2);
      const char* v1 = reinterpret_cast<const char*>(vc) + (size_t)pp1[j] * (BS * HKV * D_ * 2);
      asm volatile(
          "s_nop 4\n\tglobal_load_dwordx4 %0, %8, %10 nt\n\tglobal_load_dwordx4 %1, %8, %10 offset:2048 nt\n\t"
          "global_load_dwordx4 %2, %9, %10 nt\n\tglobal_load_dwordx4 %3, %9, %10 offset:2048 nt\n\t"
          "global_load_dwordx4 %4, %8, %11 nt\n\tglobal_load_dwordx4 %5, %8, %11 offset:2048 nt\n\t"
          "global_load_dwordx4 %6, %9, %11 nt\n\tglobal_load_dwordx4 %7, %9, %11 offset:2048 nt"
          : "=&v"(vv[j][0]), "=&v"(vv[j][1]), "=&v"(vv[j][2]), "=&v"(vv[j][3]), "=&v"(vv[j][4]), "=&v"(vv[j][5]),
            "=&v"(vv[j][6]), "=&v"(vv[j][7])
          : "v"(voff), "v"(voff2), "s"(v0), "s"(v1)
          : "memory");
    }
  }
  if (nj == 0) asm volatile("s_waitcnt vmcnt(0)" ::: "memory");
  else if (nj == 1) asm volatile("s_waitcnt vmcnt(8)" ::: "memory");
  else if (nj == 2) asm volatile("s_waitcnt vmcnt(16)" ::: "memory");
  else if (nj == 3) asm volatile("s_waitcnt vmcnt(24)" ::: "memory");
  else asm volatile("s_waitcnt vmcnt(32)" ::: "memory");
  asm volatile("" : "+v"(qb[0]), "+v"(qb[1]), "+v"(qb[2]), "+v"(qb[3])::"memory");
  __builtin_amdgcn_sched_barrier(0);

  float x[MAXJ][8];
  float m = -INFINITY;
#pragma unroll
  for (int j = 0; j < MAXJ; ++j) {
    if (j < nj) {
#pragma unroll
      for (int h2 = 0; h2 < 2; ++h2) {
        f32x4 a = {0.f, 0.f, 0.f, 0.f};
#pragma unroll
        for (int s = 0; s < 4; ++s) {
          const u32x4 ka =
              *reinterpret_cast<const u32x4*>(wl + j * (NWAVE * REG_LDS) + h2 * 4096 + c * 256 + (((4 * s + g) ^ c) * 16));
          a = __builtin_amdgcn_mfma_f32_16x16x32_bf16(__builtin_bit_cast(bf16x8, ka), __builtin_bit_cast(bf16x8, qb[s]),
                                                      a, 0, 0, 0);
        }
#pragma unroll
        for (int r4 = 0; r4 < 4; ++r4) {
          x[j][h2 * 4 + r4] = (h2 * 16 + r4 < rem[j]) ? a[r4] * scl : -INFINITY;
          m = fmaxf(m, x[j][h2 * 4 + r4]);
        }
      }
    }
  }
  m = xg_max(m);
  const float msub = (m == -INFINITY) ? 0.f : m;
  float l = 0.f;
  u32x4 pa[MAXJ];
#pragma unroll
  for (int j = 0; j < MAXJ; ++j) {
    if (j < nj) {
      float p[8];
#pragma unroll
      for (int i = 0; i < 8; ++i) p[i] = __builtin_amdgcn_exp2f(x[j][i] - msub);
      l += ((p[0] + p[1]) + (p[2] + p[3])) + ((p[4] + p[5]) + (p[6] + p[7]));
      pa[j] = u32x4{cvtpk(p[0], p[1]), cvtpk(p[2], p[3]), cvtpk(p[4], p[5]), cvtpk(p[6], p[7])};
    }
  }
  f32x4 o[8];
#pragma unroll
  for (int e = 0; e < 8; ++e) o[e] = f32x4{0.f, 0.f, 0.f, 0.f};
#pragma unroll
  for (int j = 0; j < MAXJ; ++j) {
    if (j < nj) {
      {
        const int later = nj - 1 - j;
        if (later == 0) asm volatile("s_waitcnt vmcnt(0)" ::: "memory");
        else if (later == 1) asm volatile("s_waitcnt vmcnt(8)" ::: "memory");
        else if (later == 2) asm volatile("s_waitcnt vmcnt(16)" ::: "memory");
        else asm volatile("s_waitcnt vmcnt(24)" ::: "memory");
      }
      asm volatile(""
                   : "+v"(vv[j][0]), "+v"(vv[j][1]), "+v"(vv[j][2]), "+v"(vv[j][3]), "+v"(vv[j][4]), "+v"(vv[j][5]),
                     "+v"(vv[j][6]), "+v"(vv[j][7])::"memory");
#pragma unroll
      for (int e = 0; e < 8; ++e) {
        const unsigned sel = (e & 1) ? 0x07060302u : 0x05040100u;
        u32x4 vb;
#pragma unroll
        for (int d = 0; d < 4; ++d) vb[d] = __builtin_amdgcn_perm(vv[j][2 * d + 1][e >> 1], vv[j][2 * d][e >> 1], sel);
        o[e] = __builtin_amdgcn_mfma_f32_16x16x32_bf16(__builtin_bit_cast(bf16x8, pa[j]), __builtin_bit_cast(bf16x8, vb),
                                                       o[e], 0, 0, 0);
      }
    }
  }
  l = xg_sum(l);
  float* lds_m = reinterpret_cast<float*>(lds + LDS_ML);
  float* lds_l = lds_m + NWP * 16;
  {
    float* wo = reinterpret_cast<float*>(wl) + g * (G_ * D_);
#pragma unroll
    for (int r4 = 0; r4 < 4; ++r4) {
      *reinterpret_cast<f32x4*>(wo + r4 * D_ + 8 * c) = f32x4{o[0][r4], o[1][r4], o[2][r4], o[3][r4]};
      *reinterpret_cast<f32x4*>(wo + r4 * D_ + 8 * c + 4) = f32x4{o[4][r4], o[5][r4], o[6][r4], o[7][r4]};
    }
    if (g == 0) {
      lds_m[c * NWP + wave] = m;
      lds_l[c * NWP + wave] = l;
    }
  }
  __syncthreads();
  if (tid < 256) {
    const int row16 = tid >> 4, d8 = tid & 15;
    float mv[NWAVE], lv[NWAVE];
    f32x4 oa[NWAVE], ob[NWAVE];
#pragma unroll
    for (int w = 0; w < NWAVE; ++w) {
      mv[w] = lds_m[row16 * NWP + w];
      lv[w] = lds_l[row16 * NWP + w];
      const char* src = lds + w * REG_LDS + (row16 * D_ + 4 * d8) * 4;
      oa[w] = *reinterpret_cast<const f32x4*>(src);
      ob[w] = *reinterpret_cast<const f32x4*>(src + 256);
    }
    float M = mv[0];
#pragma unroll
    for (int w = 1; w < NWAVE; ++w) M = fmaxf(M, mv[w]);
    const float Ms = (M == -INFINITY) ? 0.f : M;
    float L = 0.f;
    f32x4 a = {0.f, 0.f, 0.f, 0.f}, bacc = {0.f, 0.f, 0.f, 0.f};
#pragma unroll
    for (int w = 0; w < NWAVE; ++w) {
      const float f = __builtin_amdgcn_exp2f(mv[w] - Ms);
      L = fmaf(f, lv[w], L);
      a += f > 0.f ? f * oa[w] : f32x4{0.f, 0.f, 0.f, 0.f};
      bacc += f > 0.f ? f * ob[w] : f32x4{0.f, 0.f, 0.f, 0.f};
    }
    const int row = (b0 + (row16 >> 2)) * H_ + kvh * G_ + (row16 & 3);
    float* dst = ws_o + ((size_t)row * NPART + cset) * D_ + 4 * d8;
    asm volatile("global_store_dwordx4 %0, %1, off sc0 sc1\n\tglobal_store_dwordx4 %0, %2, off offset:256 sc0 sc1\n\ts_nop 1" ::"v"(dst), "v"(a), "v"(bacc) : "memory");
    if (d8 == 0) *reinterpret_cast<float2*>(ws_ml + (row * NPART + cset) * 2) = make_float2(M, L);
  }
}

template <int CTRL>
__device__ __forceinline__ float dpp_f(float x) {
  return __builtin_bit_cast(float, __builtin_amdgcn_mov_dpp(__builtin_bit_cast(int, x), CTRL, 0xf, 0xf, true));
}

__global__ void __launch_bounds__(64)
attn_combine(const float* __restrict__ ws_o, const float* __restrict__ ws_ml, unsigned* __restrict__ out) {
  static_assert(NPART == 16, "one partial per lane of a 16-lane row");
  const int row = blockIdx.x;
  const int lane = threadIdx.x;
  const float2 ml = *reinterpret_cast<const float2*>(ws_ml + (row * NPART + (lane & 15)) * 2);
  float2 oo[NPART];
#pragma unroll
  for (int s = 0; s < NPART; ++s)
    oo[s] = *reinterpret_cast<const float2*>(ws_o + ((size_t)row * NPART + s) * D_ + 2 * lane);
  float M = ml.x;
  M = fmaxf(M, dpp_f<0x128>(M));
  M = fmaxf(M, dpp_f<0x124>(M));
  M = fmaxf(M, dpp_f<0x122>(M));
  M = fmaxf(M, dpp_f<0x121>(M));
  const float f = __builtin_amdgcn_exp2f(ml.x - M);
  float L = f * ml.y;
  L += dpp_f<0x128>(L);
  L += dpp_f<0x124>(L);
  L += dpp_f<0x122>(L);
  L += dpp_f<0x121>(L);
  const float w = f / L;
  float a0 = 0.f, a1 = 0.f;
#pragma unroll
  for (int s = 0; s < NPART; ++s) {
    const float ws = __builtin_bit_cast(float, __builtin_amdgcn_readlane(__builtin_bit_cast(int, w), s));
    a0 = fmaf(ws, oo[s].x, a0);
    a1 = fmaf(ws, oo[s].y, a1);
  }
  out[row * (D_ / 2) + lane] = cvtpk(a0, a1);
}

}

extern "C" void kernel_launch(void* const* d_in, const int* in_sizes, int n_in, void* d_out, int out_size,
                              void* d_ws, size_t ws_size, hipStream_t stream) {
  const uint16_t* q = (const uint16_t*)d_in[0];
  const uint16_t* kc = (const uint16_t*)d_in[1];
  const uint16_t* vc = (const uint16_t*)d_in[2];
  const int* bt = (const int*)d_in[3];
  const int* ctxl = (const int*)d_in[4];
  float* ws_o = (float*)d_ws;
  float* ws_ml = ws_o + (size_t)B_ * H_ * NPART * D_;
  attn_main<<<HKV * (B_ / NQ) * NPART, NWAVE * 64, 0, stream>>>(q, kc, vc, bt, ctxl, ws_o, ws_ml);
  attn_combine<<<B_ * H_, 64, 0, stream>>>(ws_o, ws_ml, (unsigned*)d_out);
}
```
